```python
import torch

def _softmax(x, axis=-1):
    return torch.softmax(x, dim=axis)

def _gelu(x):
    return torch.nn.functional.gelu(x, approximate='tanh')

def _silu(x):
    return torch.nn.functional.silu(x)

def _sigmoid(x):
    return torch.sigmoid(x)

def _layernorm(x, axis=-1, eps=1e-05):
    m = x.mean(dim=axis, keepdim=True)
    v = x.var(dim=axis, keepdim=True, unbiased=False)
    return (x - m) / torch.sqrt(v + eps)

def _scatter_add(tgt, idx, src, axis=0):
    return tgt.clone().index_add_(axis, idx, src)

def _segment_sum(src, seg, n):
    out = torch.zeros((n,) + src.shape[1:], dtype=src.dtype)
    return out.index_add_(0, seg, src)

def _pad(x, pw):
    flat = tuple((v for pair in reversed(pw) for v in pair))
    return torch.nn.functional.pad(x, flat)
kernel_name = 'standalone__tree_speculative_verify_tree_attention_over_candidate_token_tree__prompt'

def setup_inputs():

    _w = lambda a: (lambda t: t.to(torch.bfloat16) if t.is_floating_point() else t)(torch.as_tensor(a))
    B, Hq, Hkv, T, C, D = (1, 32, 8, 2048, 2048, 128)
    tree_mask = torch.zeros((B, T, T), dtype=float)
    for b in range(B):
        parent = torch.zeros(T, dtype=torch.int64)
        for i in range(1, T):
            parent[i] = int(torch.randint(0, i, (1,)).item())
        for i in range(T):
            j = i
            tree_mask[b, i, j] = 1.0
            while j > 0:
                j = parent[j]
                tree_mask[b, i, j] = 1.0
    return {'q': _w((lambda *s: torch.randn(*s, dtype=torch.bfloat16) * 0.8)(B, Hq, T, D)), 'k_cache': _w((lambda *s: torch.randn(*s, dtype=torch.bfloat16) * 0.8)(B, Hkv, C, D)), 'v_cache': _w((lambda *s: torch.randn(*s, dtype=torch.bfloat16) * 0.05)(B, Hkv, C, D)), 'k_tree': _w((lambda *s: torch.randn(*s, dtype=torch.bfloat16) * 0.8)(B, Hkv, T, D)), 'v_tree': _w((lambda *s: torch.randn(*s, dtype=torch.bfloat16) * 0.05)(B, Hkv, T, D)), 'tree_mask': _w(tree_mask)}

def reference(q, k_cache, v_cache, k_tree, v_tree, tree_mask):
    B, Hq, T, D = q.shape
    Hkv, C = (k_cache.shape[1], k_cache.shape[2])
    rep = Hq // Hkv
    kc = (lambda x, n, axis: torch.repeat_interleave(x, n, dim=axis))(k_cache, rep, axis=1)
    vc = (lambda x, n, axis: torch.repeat_interleave(x, n, dim=axis))(v_cache, rep, axis=1)
    kt = (lambda x, n, axis: torch.repeat_interleave(x, n, dim=axis))(k_tree, rep, axis=1)
    vt = (lambda x, n, axis: torch.repeat_interleave(x, n, dim=axis))(v_tree, rep, axis=1)
    k = torch.concatenate([kc, kt], axis=2)
    v = torch.concatenate([vc, vt], axis=2)
    scale = D ** (-0.5)
    scores = torch.einsum('bhqd,bhkd->bhqk', q, k) * scale
    ctx_mask = torch.ones((B, 1, T, C), dtype=scores.dtype)
    tm = tree_mask[:, None, :, :].clone()
    full_mask = torch.concatenate([ctx_mask, tm], axis=-1)
    scores = scores + (1.0 - full_mask) * -1000000000.0
    p = _softmax(scores, axis=-1)
    return torch.einsum('bhqk,bhkd->bhqd', p, v)
```

```cpp
#include <hip/hip_runtime.h>
#include <cstdio>
#include <utility>

constexpr int D = 128, NWAVE = 4, KVBLK = 64;
constexpr float THR = 8.0f;
constexpr int HQ = 32, HKV = 8, TQ = 2048, CC = 2048, NT = CC / KVBLK;
constexpr unsigned KRS = 256u, KTILEB = 64u * KRS;
constexpr size_t SHM_ATTN = 163840;
constexpr int LCAP = 8 * TQ;
constexpr size_t WS_CNT = 0, WS_ENT = 4096;

typedef float    f32x16 __attribute__((ext_vector_type(16)));
typedef float    f32x2  __attribute__((ext_vector_type(2)));
typedef float    f32x4  __attribute__((ext_vector_type(4)));
typedef __bf16   bf16x8 __attribute__((ext_vector_type(8)));
typedef __bf16   bf16x4 __attribute__((ext_vector_type(4)));
typedef __bf16   bf16x2 __attribute__((ext_vector_type(2)));
typedef unsigned u32x4  __attribute__((ext_vector_type(4)));
typedef unsigned u32x2  __attribute__((ext_vector_type(2)));
template <int... Is> using iseq = std::integer_sequence<int, Is...>;
template <int N> using make_iseq = std::make_integer_sequence<int, N>;

#define KRING 0x0000u
#define VRING 0x8000u
#define QSTAGE 0x10000u
#define OSTAGE 0x20000u
#define SRD_WORD3 0x00020000
#define DI __device__ __forceinline__
#define GAP() __builtin_amdgcn_sched_barrier(0)
#define W4_ACLOB_HI "a128","a129","a130","a131","a132","a133","a134","a135","a136","a137","a138","a139","a140","a141","a142","a143","a144","a145","a146","a147","a148","a149","a150","a151","a152","a153","a154","a155","a156","a157","a158","a159","a160","a161","a162","a163","a164","a165","a166","a167","a168","a169","a170","a171","a172","a173","a174","a175","a176","a177","a178","a179","a180","a181","a182","a183","a184","a185","a186","a187","a188","a189","a190","a191","a192","a193","a194","a195","a196","a197","a198","a199","a200","a201","a202","a203","a204","a205","a206","a207","a208","a209","a210","a211","a212","a213","a214","a215","a216","a217","a218","a219","a220","a221","a222","a223","a224","a225","a226","a227","a228","a229","a230","a231","a232","a233","a234","a235","a236","a237","a238","a239","a240","a241","a242","a243","a244","a245","a246","a247","a248","a249","a250","a251","a252","a253","a254","a255"
#define W4_ACLOB "a0","a1","a2","a3","a4","a5","a6","a7","a8","a9","a10","a11","a12","a13","a14","a15","a16","a17","a18","a19","a20","a21","a22","a23","a24","a25","a26","a27","a28","a29","a30","a31","a32","a33","a34","a35","a36","a37","a38","a39","a40","a41","a42","a43","a44","a45","a46","a47","a48","a49","a50","a51","a52","a53","a54","a55","a56","a57","a58","a59","a60","a61","a62","a63","a64","a65","a66","a67","a68","a69","a70","a71","a72","a73","a74","a75","a76","a77","a78","a79","a80","a81","a82","a83","a84","a85","a86","a87","a88","a89","a90","a91","a92","a93","a94","a95","a96","a97","a98","a99","a100","a101","a102","a103","a104","a105","a106","a107","a108","a109","a110","a111","a112","a113","a114","a115","a116","a117","a118","a119","a120","a121","a122","a123","a124","a125","a126","a127"

DI unsigned rfl(unsigned x) { return __builtin_amdgcn_readfirstlane(x); }
DI unsigned cvtpk(float lo, float hi) {
  f32x2 v = {lo, hi}; bf16x2 b = __builtin_convertvector(v, bf16x2); return __builtin_bit_cast(unsigned, b);
}
DI float max3f(float a, float b, float c) { float r; asm("v_max3_f32 %0, %1, %2, %3" : "=v"(r) : "v"(a), "v"(b), "v"(c)); return r; }
DI float max2f(float a, float b) { float r; asm("v_max_f32_e32 %0, %1, %2" : "=v"(r) : "v"(a), "v"(b)); return r; }
DI float fadd_s(float a, float b) { float r; asm("v_add_f32_e32 %0, %1, %2" : "=v"(r) : "v"(a), "v"(b)); return r; }
DI float fmul_s(float a, float b) { float r; asm("v_mul_f32_e32 %0, %1, %2" : "=v"(r) : "v"(a), "v"(b)); return r; }
DI float fsub_s(float a, float b) { float r; asm("v_sub_f32_e32 %0, %1, %2" : "=v"(r) : "v"(a), "v"(b)); return r; }
DI float w4_aone() { unsigned l; asm volatile("v_mbcnt_lo_u32_b32 %0, -1, 0\n\tv_mbcnt_hi_u32_b32 %0, -1, %0" : "=v"(l)); return l < 32u ? 1.f : 0.f; }
DI void negm_bcast(f32x16& negm, float aone, float neg_mhat) {
  asm volatile("s_nop 1\n\tv_mfma_f32_32x32x2_f32 %0, %1, %2, 0" : "+v"(negm) : "v"(aone), "v"(neg_mhat));
}

struct W4C {
  unsigned vkb[4], vvb, vksrc[2], vvsrc, m0k, m0v, sofk, sofv, ldsb;
  __amdgpu_buffer_rsrc_t srdK, srdV;
  int T; float THR;
  f32x16 S[2][2][2];
  f32x16 NEGM[2];
  bf16x8 Vf[4][4];
  bf16x8 Pk[2][4];
  bf16x4 vlo[4][4], vup[4][4];
  float mhat[2], lsum[2], fct[2]; bool pend;
  float rs0[2], rs1[2], pmx0[2], pmx1[2], rmax[2], delta[2];
  unsigned pm0[8], psf[8];
};

constexpr int AO_(int db, int qb) { return 16 * (2 * db + qb); }
constexpr int AQ_(int qb, int d0) { return 128 + 32 * qb + 4 * d0; }
constexpr int AK_(int kb, int d0) { return 192 + 32 * kb + 4 * d0; }

template <int I> DI void tr_read(W4C& c, unsigned vslot) {
  constexpr int ks = I >> 3, db = (I >> 1) & 3;
  if constexpr ((I & 1) == 0) asm volatile("ds_read_b64_tr_b16 %0, %1 offset:%2" : "=v"(c.vlo[db][ks]) : "v"(c.vvb + vslot), "i"(4096 * ks + 512 * db) : "memory");
  else                        asm volatile("ds_read_b64_tr_b16 %0, %1 offset:%2" : "=v"(c.vup[db][ks]) : "v"(c.vvb + vslot), "i"(4096 * ks + 2048 + 512 * db) : "memory");
}
template <int I> DI void k_read(W4C& c, unsigned slot) {
  constexpr int kb = I >> 3, d0 = I & 7;
  asm volatile("ds_read_b128 a[%c1:%c2], %0 offset:%c3" :: "v"(c.vkb[d0 & 3] + slot), "i"(AK_(kb, d0)), "i"(AK_(kb, d0) + 3), "i"(8192 * kb + 128 * (d0 >> 2)) : "memory", W4_ACLOB_HI);
}
DI bf16x8 q_ldread(unsigned base, const int qb, const int d0) {
  bf16x8 r;
  switch (2 * qb + (d0 >> 2)) {
    case 0: asm volatile("ds_read_b128 %0, %1 offset:0"    : "=v"(r) : "v"(base) : "memory"); break;
    case 1: asm volatile("ds_read_b128 %0, %1 offset:128"  : "=v"(r) : "v"(base) : "memory"); break;
    case 2: asm volatile("ds_read_b128 %0, %1 offset:8192" : "=v"(r) : "v"(base) : "memory"); break;
    default: asm volatile("ds_read_b128 %0, %1 offset:8320" : "=v"(r) : "v"(base) : "memory"); break;
  }
  return r;
}
template <int I> DI void q_write1(const u32x4& w) {
  constexpr int aq = AQ_(I >> 3, I & 7);
  asm volatile("v_accvgpr_write_b32 a[%c4], %0\n\tv_accvgpr_write_b32 a[%c5], %1\n\tv_accvgpr_write_b32 a[%c6], %2\n\tv_accvgpr_write_b32 a[%c7], %3"
               :: "v"(w[0]), "v"(w[1]), "v"(w[2]), "v"(w[3]), "i"(aq), "i"(aq + 1), "i"(aq + 2), "i"(aq + 3) : W4_ACLOB_HI);
}
DI void q_write(const int qb, const int d0, const u32x4& w) {
  switch (8 * qb + d0) {
    case 0: q_write1<0>(w); break;   case 1: q_write1<1>(w); break;   case 2: q_write1<2>(w); break;   case 3: q_write1<3>(w); break;
    case 4: q_write1<4>(w); break;   case 5: q_write1<5>(w); break;   case 6: q_write1<6>(w); break;   case 7: q_write1<7>(w); break;
    case 8: q_write1<8>(w); break;   case 9: q_write1<9>(w); break;   case 10: q_write1<10>(w); break; case 11: q_write1<11>(w); break;
    case 12: q_write1<12>(w); break; case 13: q_write1<13>(w); break; case 14: q_write1<14>(w); break; case 15: q_write1<15>(w); break;
  }
}
template <int A0> DI void read_o16(float (&o)[16]) {
  asm volatile("v_accvgpr_read_b32 %0, a[%c16]\n\tv_accvgpr_read_b32 %1, a[%c17]\n\tv_accvgpr_read_b32 %2, a[%c18]\n\tv_accvgpr_read_b32 %3, a[%c19]\n\t"
               "v_accvgpr_read_b32 %4, a[%c20]\n\tv_accvgpr_read_b32 %5, a[%c21]\n\tv_accvgpr_read_b32 %6, a[%c22]\n\tv_accvgpr_read_b32 %7, a[%c23]\n\t"
               "v_accvgpr_read_b32 %8, a[%c24]\n\tv_accvgpr_read_b32 %9, a[%c25]\n\tv_accvgpr_read_b32 %10, a[%c26]\n\tv_accvgpr_read_b32 %11, a[%c27]\n\t"
               "v_accvgpr_read_b32 %12, a[%c28]\n\tv_accvgpr_read_b32 %13, a[%c29]\n\tv_accvgpr_read_b32 %14, a[%c30]\n\tv_accvgpr_read_b32 %15, a[%c31]"
               : "=v"(o[0]), "=v"(o[1]), "=v"(o[2]), "=v"(o[3]), "=v"(o[4]), "=v"(o[5]), "=v"(o[6]), "=v"(o[7]),
                 "=v"(o[8]), "=v"(o[9]), "=v"(o[10]), "=v"(o[11]), "=v"(o[12]), "=v"(o[13]), "=v"(o[14]), "=v"(o[15])
               : "i"(A0), "i"(A0 + 1), "i"(A0 + 2), "i"(A0 + 3), "i"(A0 + 4), "i"(A0 + 5), "i"(A0 + 6), "i"(A0 + 7),
                 "i"(A0 + 8), "i"(A0 + 9), "i"(A0 + 10), "i"(A0 + 11), "i"(A0 + 12), "i"(A0 + 13), "i"(A0 + 14), "i"(A0 + 15));
}
template <int A0> DI void scale_o16(float f) {
  float t0, t1;
  asm volatile("v_accvgpr_read_b32 %0, a[%c3]\n\tv_accvgpr_read_b32 %1, a[%c4]\n\tv_mul_f32 %0, %2, %0\n\tv_mul_f32 %1, %2, %1\n\tv_accvgpr_write_b32 a[%c3], %0\n\tv_accvgpr_write_b32 a[%c4], %1"
               : "=&v"(t0), "=&v"(t1) : "v"(f), "i"(A0), "i"(A0 + 1) : W4_ACLOB);
}
template <int... Is> DI void scale_group(float f0, float f1, iseq<Is...>) {
  (scale_o16<2 * Is>((((2 * Is) >> 4) & 1) ? f1 : f0), ...);
}
DI void rescale_O(W4C& c) {
  asm volatile("s_nop 7\n\ts_nop 7" ::: "memory");
  scale_group(c.fct[0], c.fct[1], make_iseq<64>{});
  asm volatile("s_nop 1" ::: "memory");
  c.lsum[0] = fmul_s(c.lsum[0], c.fct[0]); c.lsum[1] = fmul_s(c.lsum[1], c.fct[1]);
}

template <int X, bool FIRST, int M> DI void qk_mfma(W4C& c) {
  constexpr int d0 = M >> 2, kb = (M >> 1) & 1, qb = M & 1, ak = AK_(kb, d0), aq = AQ_(qb, d0);
  if constexpr (d0 == 0) {
    if constexpr (FIRST) asm volatile("v_mfma_f32_32x32x16_bf16 %0, a[%c1:%c2], a[%c3:%c4], 0" : "=&v"(c.S[X][kb][qb]) : "i"(ak), "i"(ak + 3), "i"(aq), "i"(aq + 3) : W4_ACLOB, W4_ACLOB_HI);
    else                 asm volatile("v_mfma_f32_32x32x16_bf16 %0, a[%c2:%c3], a[%c4:%c5], %1" : "=&v"(c.S[X][kb][qb]) : "v"(c.NEGM[qb]), "i"(ak), "i"(ak + 3), "i"(aq), "i"(aq + 3) : W4_ACLOB, W4_ACLOB_HI);
  } else asm volatile("v_mfma_f32_32x32x16_bf16 %0, a[%c1:%c2], a[%c3:%c4], %0" : "+v"(c.S[X][kb][qb]) : "i"(ak), "i"(ak + 3), "i"(aq), "i"(aq + 3) : W4_ACLOB, W4_ACLOB_HI);
}
template <int M, bool ZERO> DI void pv_mfma(W4C& c) {
  constexpr int ks = M >> 3, db = (M >> 1) & 3, qb = M & 1, ao = AO_(db, qb);
  if constexpr (ZERO && ks == 0) asm volatile("v_mfma_f32_32x32x16_bf16 a[%c2:%c3], %0, %1, 0" :: "v"(c.Vf[db][ks]), "v"(c.Pk[qb][ks]), "i"(ao), "i"(ao + 15) : W4_ACLOB);
  else                         asm volatile("v_mfma_f32_32x32x16_bf16 a[%c2:%c3], %0, %1, a[%c2:%c3]" :: "v"(c.Vf[db][ks]), "v"(c.Pk[qb][ks]), "i"(ao), "i"(ao + 15) : W4_ACLOB);
}
template <int X, int QB, int KS, int P> DI void pack1(W4C& c) {
  u32x4 w = __builtin_bit_cast(u32x4, c.Pk[QB][KS]);
  w[P] = cvtpk(c.S[X][KS >> 1][QB][8 * (KS & 1) + 2 * P], c.S[X][KS >> 1][QB][8 * (KS & 1) + 2 * P + 1]);
  c.Pk[QB][KS] = __builtin_bit_cast(bf16x8, w);
}
template <int X, int J> DI void rowsum1(W4C& c) {
  constexpr int qb = J >> 4, ks = (J >> 2) & 3, p = J & 3;
  c.rs0[qb] = fadd_s(c.rs0[qb], c.S[X][ks >> 1][qb][8 * (ks & 1) + 2 * p]); c.rs1[qb] = fadd_s(c.rs1[qb], c.S[X][ks >> 1][qb][8 * (ks & 1) + 2 * p + 1]);
}

template <int X, bool FIRST, int M> DI void gapA(W4C& c, unsigned vslot, const int t) {
  constexpr int Xp = X ^ 1;
  qk_mfma<X, FIRST, M>(c);
  if constexpr (M >= 16 && M <= 31) {
    const int tk = t + 2, tv = t + 1;
    constexpr int q = (M - 16) >> 1;
    if constexpr (((M - 16) & 1) == 0) {
      if constexpr (q < 4) c.pm0[q] = c.m0k + (unsigned)((t + 2) & 1) * 0x4000u + 1024u * q; else c.pm0[q] = c.m0v + (unsigned)((t + 1) & 1) * 0x4000u + 1024u * (q - 4);
      asm volatile("" : "+s"(c.pm0[q]));
    } else {
      if constexpr (q < 4) c.psf[q] = KTILEB * (unsigned)tk + c.sofk + KRS * 4u * q; else c.psf[q] = KTILEB * (unsigned)tv + c.sofv + KRS * 8u * ((q - 4) >> 1) + 128u * ((q - 4) & 1);
      asm volatile("" : "+s"(c.psf[q]));
    }
  }
  if constexpr (M == 31) asm volatile("s_nop 4" : "+s"(c.pm0[0]), "+s"(c.pm0[1]), "+s"(c.pm0[2]), "+s"(c.pm0[3]), "+s"(c.pm0[4]), "+s"(c.pm0[5]), "+s"(c.pm0[6]), "+s"(c.pm0[7]),
                                                  "+s"(c.psf[0]), "+s"(c.psf[1]), "+s"(c.psf[2]), "+s"(c.psf[3]), "+s"(c.psf[4]), "+s"(c.psf[5]), "+s"(c.psf[6]), "+s"(c.psf[7]));
  if constexpr (!FIRST && M == 8) asm volatile("" :: "v"(c.NEGM[0]), "v"(c.NEGM[1]));
  if constexpr (!FIRST) {
    constexpr int base = M + (M >= 4) + (M >= 8) + (M >= 12) + (M >= 16) + (M >= 20) + (M >= 24);
    if constexpr (M <= 25) tr_read<base>(c, vslot);
    if constexpr (M == 3 || M == 7 || M == 11 || M == 15 || M == 19 || M == 23) tr_read<base + 1>(c, vslot);
    if constexpr (M < 16) {
      constexpr int e0 = 2 * M, e1 = 2 * M + 1;
      c.S[Xp][1][e0 >> 4][e0 & 15] = __builtin_amdgcn_exp2f(c.S[Xp][1][e0 >> 4][e0 & 15]);
      c.S[Xp][1][e1 >> 4][e1 & 15] = __builtin_amdgcn_exp2f(c.S[Xp][1][e1 >> 4][e1 & 15]);
      pack1<Xp, (M >> 3), ((M >> 2) & 1), (M & 3)>(c);
    } else {
      constexpr int cc = M - 16;
      pack1<Xp, (cc >> 3), 2 + ((cc >> 2) & 1), (cc & 3)>(c);
      rowsum1<Xp, 16 * (cc >> 3) + 8 + (4 * ((cc >> 2) & 1)) + (cc & 3)>(c);
    }
  }
  GAP();
}
#define W4_VLO "+v"(c.vlo[0][0]), "+v"(c.vlo[0][1]), "+v"(c.vlo[0][2]), "+v"(c.vlo[0][3]), "+v"(c.vlo[1][0]), "+v"(c.vlo[1][1]), "+v"(c.vlo[1][2]), "+v"(c.vlo[1][3]), \
               "+v"(c.vlo[2][0]), "+v"(c.vlo[2][1]), "+v"(c.vlo[2][2]), "+v"(c.vlo[2][3]), "+v"(c.vlo[3][0]), "+v"(c.vlo[3][1]), "+v"(c.vlo[3][2]), "+v"(c.vlo[3][3])
#define W4_VUP "+v"(c.vup[0][0]), "+v"(c.vup[0][1]), "+v"(c.vup[0][2]), "+v"(c.vup[0][3]), "+v"(c.vup[1][0]), "+v"(c.vup[1][1]), "+v"(c.vup[1][2]), "+v"(c.vup[1][3]), \
               "+v"(c.vup[2][0]), "+v"(c.vup[2][1]), "+v"(c.vup[2][2]), "+v"(c.vup[2][3]), "+v"(c.vup[3][0]), "+v"(c.vup[3][1]), "+v"(c.vup[3][2]), "+v"(c.vup[3][3])
template <int X, bool FIRST, int... Ms> DI void phaseA_(W4C& c, const int t, iseq<Ms...>) {
  const unsigned vslot = (unsigned)((t - 1) & 1) * 0x4000u;
  (gapA<X, FIRST, Ms>(c, vslot, t), ...);
  if constexpr (!FIRST) {
    #pragma unroll
    for (int qb = 0; qb < 2; ++qb) {
      const float rs = fadd_s(c.rs0[qb], c.rs1[qb]);
      auto pr = __builtin_amdgcn_permlane32_swap(__float_as_uint(rs), __float_as_uint(rs), false, false);
      c.lsum[qb] = fadd_s(c.lsum[qb], fadd_s(__uint_as_float(pr[0]), __uint_as_float(pr[1])));
    }
  }
  if constexpr (FIRST) asm volatile("s_waitcnt vmcnt(0) lgkmcnt(0)\n\ts_barrier" ::: "memory");
  else { asm volatile("s_waitcnt vmcnt(0) lgkmcnt(0)" : W4_VLO :: "memory"); asm volatile("s_barrier" : W4_VUP :: "memory"); }
  GAP();
  if constexpr (!FIRST) {
    #pragma unroll
    for (int ks = 0; ks < 4; ++ks)
      #pragma unroll
      for (int db = 0; db < 4; ++db) c.Vf[db][ks] = __builtin_shufflevector(c.vlo[db][ks], c.vup[db][ks], 0, 1, 2, 3, 4, 5, 6, 7);
  }
}
template <int X, bool FIRST> DI void phaseA(W4C& c, const int t) { phaseA_<X, FIRST>(c, t, make_iseq<32>{}); }

template <int X, bool PEEL, bool ZERO1, int DK, int DV, bool KR, int M> DI void gapB(W4C& c, const unsigned kslot_rd) {
  if constexpr (!PEEL) pv_mfma<M, ZERO1>(c);
  if constexpr (M < 4 && DK == 1)             asm volatile("s_mov_b32 m0, %0\n\ts_nop 0\n\tbuffer_load_dwordx4 %1, %2, %3 offen lds" :: "s"(c.pm0[M]), "v"(c.vksrc[M & 1]), "s"(c.srdK),  "s"(c.psf[M]) : "m0", "memory");
  else if constexpr (M >= 4 && M < 8 && DV == 1) asm volatile("s_mov_b32 m0, %0\n\ts_nop 0\n\tbuffer_load_dwordx4 %1, %2, %3 offen lds" :: "s"(c.pm0[M]), "v"(c.vvsrc), "s"(c.srdV),  "s"(c.psf[M]) : "m0", "memory");
  if constexpr (M >= 1 && M <= 16 && KR) k_read<M - 1>(c, kslot_rd);
  if constexpr (M >= 8 && M < 16) {
    constexpr int i = M - 8, q = i >> 2, r = 4 * (i & 3);
    if constexpr ((i & 3) == 0) { c.pmx0[q] = max3f(c.S[X][0][q][0], c.S[X][0][q][1], c.S[X][1][q][0]); c.pmx1[q] = max3f(c.S[X][0][q][2], c.S[X][0][q][3], c.S[X][1][q][1]);
                                  c.pmx0[q] = max3f(c.pmx0[q], c.S[X][1][q][2], c.S[X][1][q][3]); }
    else                        { c.pmx0[q] = max3f(c.pmx0[q], c.S[X][0][q][r], c.S[X][0][q][r + 1]); c.pmx1[q] = max3f(c.pmx1[q], c.S[X][0][q][r + 2], c.S[X][0][q][r + 3]);
                                  c.pmx0[q] = max3f(c.pmx0[q], c.S[X][1][q][r], c.S[X][1][q][r + 1]); c.pmx1[q] = max3f(c.pmx1[q], c.S[X][1][q][r + 2], c.S[X][1][q][r + 3]); }
  }
  if constexpr (M == 16 || M == 17) {
    constexpr int q = M - 16; const float mm = max2f(c.pmx0[q], c.pmx1[q]);
    auto pr = __builtin_amdgcn_permlane32_swap(__float_as_uint(mm), __float_as_uint(mm), false, false);
    c.rmax[q] = max2f(__uint_as_float(pr[0]), __uint_as_float(pr[1]));
  }
  if constexpr (M == 18) {
    if constexpr (PEEL) {
      #pragma unroll
      for (int q = 0; q < 2; ++q) { c.mhat[q] = c.rmax[q]; c.lsum[q] = 0.f; c.fct[q] = 1.f;
        #pragma unroll
        for (int kb = 0; kb < 2; ++kb) { _Pragma("unroll") for (int e = 0; e < 16; ++e) c.S[X][kb][q][e] = fsub_s(c.S[X][kb][q][e], c.rmax[q]); }
        negm_bcast(c.NEGM[q], w4_aone(), -c.mhat[q]);
      }
    } else {
      const bool grew = (c.rmax[0] > c.THR) | (c.rmax[1] > c.THR);
      if (__builtin_expect(__any((int)grew), 0)) {
        #pragma unroll
        for (int q = 0; q < 2; ++q) { const float dl = max2f(c.rmax[q], 0.f); c.delta[q] = dl; c.mhat[q] = fadd_s(c.mhat[q], dl); c.fct[q] = __builtin_amdgcn_exp2f(-dl);
          #pragma unroll
          for (int kb = 0; kb < 2; ++kb) { _Pragma("unroll") for (int e = 0; e < 16; ++e) c.S[X][kb][q][e] = fsub_s(c.S[X][kb][q][e], dl); }
          negm_bcast(c.NEGM[q], w4_aone(), -c.mhat[q]);
        }
        c.pend = true;
      }
    }
  }
  if constexpr (M == 18) { c.rs0[0] = c.rs0[1] = c.rs1[0] = c.rs1[1] = 0.f; }
  if constexpr (M >= 19) {
    constexpr int g = M - 19, e_lo = (32 * g) / 13, e_hi = (32 * (g + 1)) / 13;
    {
      constexpr int p_lo = g ? (32 * (g - 1)) / 13 : 0, p_hi = g ? (32 * g) / 13 : 0;
      if constexpr (p_lo + 0 < p_hi) { constexpr int e = p_lo + 0; if constexpr (e & 1) c.rs1[e >> 4] = fadd_s(c.rs1[e >> 4], c.S[X][0][e >> 4][e & 15]); else c.rs0[e >> 4] = fadd_s(c.rs0[e >> 4], c.S[X][0][e >> 4][e & 15]); }
      if constexpr (p_lo + 1 < p_hi) { constexpr int e = p_lo + 1; if constexpr (e & 1) c.rs1[e >> 4] = fadd_s(c.rs1[e >> 4], c.S[X][0][e >> 4][e & 15]); else c.rs0[e >> 4] = fadd_s(c.rs0[e >> 4], c.S[X][0][e >> 4][e & 15]); }
      if constexpr (p_lo + 2 < p_hi) { constexpr int e = p_lo + 2; if constexpr (e & 1) c.rs1[e >> 4] = fadd_s(c.rs1[e >> 4], c.S[X][0][e >> 4][e & 15]); else c.rs0[e >> 4] = fadd_s(c.rs0[e >> 4], c.S[X][0][e >> 4][e & 15]); }
    }
    if constexpr (e_lo + 0 < e_hi) c.S[X][0][(e_lo + 0) >> 4][(e_lo + 0) & 15] = __builtin_amdgcn_exp2f(c.S[X][0][(e_lo + 0) >> 4][(e_lo + 0) & 15]);
    if constexpr (e_lo + 1 < e_hi) c.S[X][0][(e_lo + 1) >> 4][(e_lo + 1) & 15] = __builtin_amdgcn_exp2f(c.S[X][0][(e_lo + 1) >> 4][(e_lo + 1) & 15]);
    if constexpr (e_lo + 2 < e_hi) c.S[X][0][(e_lo + 2) >> 4][(e_lo + 2) & 15] = __builtin_amdgcn_exp2f(c.S[X][0][(e_lo + 2) >> 4][(e_lo + 2) & 15]);
  }
  if constexpr (M == 31) asm volatile("" :: "v"(c.S[X][0][0]), "v"(c.S[X][0][1]), "v"(c.NEGM[0]), "v"(c.NEGM[1]), "v"(c.rs0[0]), "v"(c.rs0[1]), "v"(c.rs1[0]), "v"(c.rs1[1]));
  GAP();
}
template <int X, bool PEEL, bool ZERO1, int DK, int DV, bool KR, int... Ms> DI void phaseB_(W4C& c, const int t, iseq<Ms...>) {
  const unsigned kslot_rd = (unsigned)((t + 1) & 1) * 0x4000u;
  if constexpr (!PEEL) asm volatile("s_nop 1" ::: "memory");
  GAP();
  (gapB<X, PEEL, ZERO1, DK, DV, KR, Ms>(c, kslot_rd), ...);
  {
    #pragma unroll
    for (int e = (32 * 12) / 13; e < 32; ++e) { if (e & 1) c.rs1[e >> 4] = fadd_s(c.rs1[e >> 4], c.S[X][0][e >> 4][e & 15]); else c.rs0[e >> 4] = fadd_s(c.rs0[e >> 4], c.S[X][0][e >> 4][e & 15]); }
  }
  if constexpr (!PEEL) {
    if (__builtin_expect(c.pend, 0)) { rescale_O(c); c.pend = false; }
  }
  asm volatile("s_waitcnt lgkmcnt(0)" ::: "memory");
  GAP();
}
template <int X, bool PEEL, bool ZERO1, int DK = 1, int DV = 1, bool KR = true> DI void phaseB(W4C& c, const int t) { phaseB_<X, PEEL, ZERO1, DK, DV, KR>(c, t, make_iseq<32>{}); }

template <int... Is> DI void k_reads(W4C& c, unsigned slot, iseq<Is...>) { (k_read<Is>(c, slot), ...); }
template <int... Is> DI void tr_reads(W4C& c, unsigned vslot, iseq<Is...>) { (tr_read<Is>(c, vslot), ...); }
template <int X, int... Cs> DI void packs(W4C& c, iseq<Cs...>) { (pack1<X, ((Cs >> 3) & 1), 2 * (Cs >> 4) + ((Cs >> 2) & 1), (Cs & 3)>(c), ...); }
template <int X, int J> DI void rowsum1_padded(W4C& c) { rowsum1<X, J>(c); asm volatile("s_nop 1"); }
template <int X, int... Js> DI void rowsums(W4C& c, iseq<Js...>) { (rowsum1_padded<X, Js>(c), ...); }
template <bool Z, int... Ms> DI void pv_all(W4C& c, iseq<Ms...>) { (pv_mfma<Ms, Z>(c), ...); }

template <int QB, int D0> DI void qk_tree(f32x16& s, const bf16x8& kf, const f32x16& negm) {
  constexpr int aq = AQ_(QB, D0);
  if constexpr (D0 == 0) asm volatile("s_nop 1\n\tv_mfma_f32_32x32x16_bf16 %0, %1, a[%c3:%c4], %2" : "=&v"(s) : "v"(kf), "v"(negm), "i"(aq), "i"(aq + 3) : W4_ACLOB_HI);
  else if constexpr (D0 == 7) asm volatile("s_nop 1\n\tv_mfma_f32_32x32x16_bf16 %0, %1, a[%c2:%c3], %0\n\ts_nop 7\n\ts_nop 7" : "+v"(s) : "v"(kf), "i"(aq), "i"(aq + 3) : W4_ACLOB_HI);
  else asm volatile("s_nop 1\n\tv_mfma_f32_32x32x16_bf16 %0, %1, a[%c2:%c3], %0" : "+v"(s) : "v"(kf), "i"(aq), "i"(aq + 3) : W4_ACLOB_HI);
}
template <int QB, int... D0s> DI void qk_tree_all(f32x16& s, const bf16x8 (&kf)[8], const f32x16& negm, iseq<D0s...>) { (qk_tree<QB, D0s>(s, kf[D0s], negm), ...); }
template <int M> DI void pv_tree(W4C& c) {
  constexpr int qb = M >> 3, s_ = (M >> 2) & 1, db = M & 3, ks = 2 * qb + s_, ao = AO_(db, qb);
  asm volatile("s_nop 1\n\tv_mfma_f32_32x32x16_bf16 a[%c2:%c3], %0, %1, a[%c2:%c3]" :: "v"(c.Vf[db][ks]), "v"(c.Pk[qb][ks]), "i"(ao), "i"(ao + 15) : W4_ACLOB);
}
template <int... Ms> DI void pv_tree_all(W4C& c, iseq<Ms...>) { (pv_tree<Ms>(c), ...); }

extern "C" __global__ __launch_bounds__(256, 1)
void attn_main(const unsigned short* __restrict__ Qg, const unsigned short* __restrict__ Kc, const unsigned short* __restrict__ Vc,
               const unsigned short* __restrict__ Kt, const unsigned short* __restrict__ Vt,
               const int* __restrict__ lcnt, const unsigned* __restrict__ lent, unsigned short* __restrict__ Og) {
  extern __shared__ __attribute__((aligned(16))) char lds[];
  const unsigned tid = threadIdx.x, lane = tid & 63u, wid = tid >> 6, l32 = lane & 31u, hi = lane >> 5;
  const float C = 0.12751743082f;
  W4C c;
  c.T = NT; c.THR = THR;
  c.ldsb = (unsigned)(size_t)lds;
  #pragma unroll
  for (int j = 0; j < 4; ++j) c.vkb[j] = c.ldsb + KRING + 256u * l32 + 16u * ((2u * j + hi) ^ (l32 & 7u));
  c.vvb = c.ldsb + VRING + (((lane & 3u) << 3) | (((lane >> 2) & 3u) << 6) | (((lane >> 4) & 1u) << 5) | (hi << 8));
  #pragma unroll
  for (int par = 0; par < 2; ++par) c.vksrc[par] = KRS * (lane >> 4) + 16u * ((lane & 15u) ^ (4u * par + (lane >> 4)));
  c.vvsrc = KRS * ((lane >> 2) & 7u) + 64u * hi + 16u * (lane & 3u);
  unsigned vqsrc[2];
  #pragma unroll
  for (int par = 0; par < 2; ++par) vqsrc[par] = (unsigned)(TQ * 256) * (lane >> 4) + 16u * ((lane & 15u) ^ (4u * par + (lane >> 4)));
  c.m0k = rfl(c.ldsb + KRING + 4096u * wid); c.m0v = rfl(c.ldsb + VRING + 4096u * wid);
  const unsigned qoff = QSTAGE + 16384u * wid;
  c.sofk = rfl(KRS * 16u * wid); c.sofv = rfl(KRS * 16u * wid);

  const unsigned ublk = blockIdx.x;
  const int kvh = (int)(ublk & 7u), rb = (int)(ublk >> 3);
  const int qrow0 = 64 * rb + 16 * (int)rfl(wid);
  const char* Kbase = (const char*)Kc + (size_t)kvh * CC * 256;
  const char* Vbase = (const char*)Vc + (size_t)kvh * CC * 256;
  c.srdK = __builtin_amdgcn_make_buffer_rsrc((void*)Kbase, (short)0, CC * 256, SRD_WORD3);
  c.srdV = __builtin_amdgcn_make_buffer_rsrc((void*)Vbase, (short)0, CC * 256, SRD_WORD3);
  auto dma_k = [&](int tk) {
    const unsigned slot = (unsigned)(tk & 1) * 0x4000u, skv = KTILEB * (unsigned)tk;
    #pragma unroll
    for (int q = 0; q < 4; ++q)
      asm volatile("s_nop 4\n\ts_mov_b32 m0, %0\n\ts_nop 0\n\tbuffer_load_dwordx4 %1, %2, %3 offen lds"
                   :: "s"(rfl(c.m0k + slot + 1024u * q)), "v"(c.vksrc[q & 1]), "s"(c.srdK), "s"(rfl(skv + c.sofk + KRS * 4u * q)) : "m0", "memory");
  };
  auto dma_v = [&](int tv) {
    const unsigned slot = (unsigned)(tv & 1) * 0x4000u, skv = KTILEB * (unsigned)tv;
    #pragma unroll
    for (int q = 0; q < 4; ++q)
      asm volatile("s_nop 4\n\ts_mov_b32 m0, %0\n\ts_nop 0\n\tbuffer_load_dwordx4 %1, %2, %3 offen lds"
                   :: "s"(rfl(c.m0v + slot + 1024u * q)), "v"(c.vvsrc), "s"(c.srdV), "s"(rfl(skv + c.sofv + KRS * 8u * (q >> 1) + 128u * (q & 1))) : "m0", "memory");
  };
  const unsigned m0q = rfl(c.ldsb + QSTAGE + 16384u * wid);
  {
    __amdgpu_buffer_rsrc_t srdQ = __builtin_amdgcn_make_buffer_rsrc((void*)Qg, (short)0, HQ * TQ * 256, SRD_WORD3);
    #pragma unroll
    for (int qp = 0; qp < 16; ++qp)
      asm volatile("s_nop 4\n\ts_mov_b32 m0, %0\n\ts_nop 0\n\tbuffer_load_dwordx4 %1, %2, %3 offen lds"
                   :: "s"(rfl(m0q + 1024u * qp)), "v"(vqsrc[qp & 1]), "s"(srdQ), "s"(rfl((unsigned)(((4 * kvh) * TQ + qrow0 + qp) * 256))) : "m0", "memory");
    dma_k(0); dma_v(0); dma_k(1);
  }
  asm volatile("s_waitcnt vmcnt(12)" ::: "memory");
  {
    unsigned qb4[4];
    #pragma unroll
    for (int j = 0; j < 4; ++j) { qb4[j] = c.vkb[j] + qoff; asm volatile("" : "+v"(qb4[j])); }
    #pragma unroll
    for (int qb = 0; qb < 2; ++qb) {
      bf16x8 qf[8];
      #pragma unroll
      for (int d0 = 0; d0 < 8; ++d0) qf[d0] = q_ldread(qb4[d0 & 3], qb, d0);
      asm volatile("s_waitcnt lgkmcnt(0)" : "+v"(qf[0]), "+v"(qf[1]), "+v"(qf[2]), "+v"(qf[3]), "+v"(qf[4]), "+v"(qf[5]), "+v"(qf[6]), "+v"(qf[7]) :: "memory");
      #pragma unroll
      for (int d0 = 0; d0 < 8; ++d0) {
        u32x4 w;
        #pragma unroll
        for (int e = 0; e < 4; ++e) w[e] = cvtpk(fmul_s((float)qf[d0][2 * e], C), fmul_s((float)qf[d0][2 * e + 1], C));
        q_write(qb, d0, w);
      }
    }
  }
  asm volatile("s_waitcnt vmcnt(0)\n\ts_barrier" ::: "memory");
  k_reads(c, 0u, make_iseq<16>{});
  asm volatile("s_waitcnt lgkmcnt(0)\n\ts_nop 1" ::: "memory");

  c.mhat[0] = c.mhat[1] = 0.f; c.lsum[0] = c.lsum[1] = 0.f; c.fct[0] = c.fct[1] = 1.f; c.pend = false;

  constexpr int T = NT;
  phaseA<0, true>(c, 0);  phaseB<0, true,  false>(c, 0);
  phaseA<1, false>(c, 1); phaseB<1, false, true>(c, 1);
  for (int t = 2; t < T - 2; t += 2) {
    phaseA<0, false>(c, t);     phaseB<0, false, false>(c, t);
    phaseA<1, false>(c, t + 1); phaseB<1, false, false>(c, t + 1);
  }
  phaseA<0, false>(c, T - 2); phaseB<0, false, false,  0,  1,  true>(c, T - 2);
  phaseA<1, false>(c, T - 1); phaseB<1, false, false,  0,  0,  false>(c, T - 1);
  {
    const unsigned vslot = (unsigned)((T - 1) & 1) * 0x4000u;
    tr_reads(c, vslot, make_iseq<32>{});
    #pragma unroll
    for (int e = 0; e < 32; ++e) c.S[1][1][e >> 4][e & 15] = __builtin_amdgcn_exp2f(c.S[1][1][e >> 4][e & 15]);
    packs<1>(c, make_iseq<32>{});
    rowsums<1>(c, iseq<8, 9, 10, 11, 12, 13, 14, 15, 24, 25, 26, 27, 28, 29, 30, 31>{});
    #pragma unroll
    for (int qb = 0; qb < 2; ++qb) { const float rs = fadd_s(c.rs0[qb], c.rs1[qb]);
      auto pr = __builtin_amdgcn_permlane32_swap(__float_as_uint(rs), __float_as_uint(rs), false, false);
      c.lsum[qb] = fadd_s(c.lsum[qb], fadd_s(__uint_as_float(pr[0]), __uint_as_float(pr[1]))); }
    asm volatile("s_waitcnt lgkmcnt(0)" : W4_VLO :: "memory"); asm volatile("" : W4_VUP :: "memory");
    GAP();
    #pragma unroll
    for (int ks = 0; ks < 4; ++ks)
      #pragma unroll
      for (int db = 0; db < 4; ++db) c.Vf[db][ks] = __builtin_shufflevector(c.vlo[db][ks], c.vup[db][ks], 0, 1, 2, 3, 4, 5, 6, 7);
    asm volatile("s_nop 1" ::: "memory");
    pv_all<false>(c, make_iseq<32>{});
    asm volatile("s_nop 7\n\ts_nop 7" ::: "memory");
  }
  {
    const int qblk0 = 8 * rb + 2 * (int)rfl(wid);
    const int cnt0 = lcnt[qblk0], cnt1 = lcnt[qblk0 + 1];
    const int ntile = ((cnt0 > cnt1 ? cnt0 : cnt1) + 31) >> 5;
    const unsigned* entp = lent + ((size_t)qblk0 + hi) * LCAP;
    const int cntl = hi ? cnt1 : cnt0;
    const char* Ktb = (const char*)Kt + (size_t)kvh * TQ * 256;
    const char* Vtb = (const char*)Vt + (size_t)kvh * TQ * 256;
    char* vimg = lds + QSTAGE + 16384u * wid;
    const unsigned vrd = QSTAGE + 16384u * wid - VRING;
    for (int j = 0; j < ntile; ++j) {
      const int idx = 32 * j + (int)l32;
      unsigned e = 0x00FF0000u;
      if (idx < cntl) e = entp[idx];
      const unsigned e0 = __shfl(e, (int)l32), e1 = __shfl(e, 32 + (int)l32);
      bf16x8 kf0[8], kf1[8];
      {
        const char* k0 = Ktb + (size_t)(e0 & 0xFFFFu) * 256 + 16u * hi;
        const char* k1 = Ktb + (size_t)(e1 & 0xFFFFu) * 256 + 16u * hi;
        #pragma unroll
        for (int d0 = 0; d0 < 8; ++d0) { kf0[d0] = *(const bf16x8*)(k0 + 32 * d0); kf1[d0] = *(const bf16x8*)(k1 + 32 * d0); }
      }
      #pragma unroll
      for (int g = 0; g < 8; ++g) {
        const unsigned ks_ = __shfl(e, 8 * g + (int)((lane >> 2) & 7u)) & 0xFFFFu;
        const char* vs = Vtb + (size_t)ks_ * 256 + 64u * hi + 16u * (lane & 3u);
        const u32x4 v0 = *(const u32x4*)vs, v1 = *(const u32x4*)(vs + 128);
        *(u32x4*)(vimg + 2048 * g + 16 * lane) = v0;
        *(u32x4*)(vimg + 2048 * g + 1024 + 16 * lane) = v1;
      }
      f32x16 s0, s1;
      qk_tree_all<0>(s0, kf0, c.NEGM[0], make_iseq<8>{});
      qk_tree_all<1>(s1, kf1, c.NEGM[1], make_iseq<8>{});
      {
        const unsigned rid = (e >> 16) & 0xFFu;
        unsigned long long mk = 0;
        #pragma unroll
        for (int r = 0; r < 8; ++r) { const unsigned long long b = __ballot(rid == (unsigned)r); if ((l32 >> 2) == (unsigned)r) mk = b; }
        const unsigned m0 = (unsigned)mk, m1 = (unsigned)(mk >> 32);
        #pragma unroll
        for (int r = 0; r < 16; ++r) {
          const unsigned sl = (r & 3) + 8 * (r >> 2) + 4 * hi;
          s0[r] = ((m0 >> sl) & 1u) ? s0[r] : -1e30f;
          s1[r] = ((m1 >> sl) & 1u) ? s1[r] : -1e30f;
        }
      }
      float rm0 = s0[0], rm1 = s1[0];
      #pragma unroll
      for (int r = 1; r < 16; ++r) { rm0 = max2f(rm0, s0[r]); rm1 = max2f(rm1, s1[r]); }
      { auto pr = __builtin_amdgcn_permlane32_swap(__float_as_uint(rm0), __float_as_uint(rm0), false, false); rm0 = max2f(__uint_as_float(pr[0]), __uint_as_float(pr[1])); }
      { auto pr = __builtin_amdgcn_permlane32_swap(__float_as_uint(rm1), __float_as_uint(rm1), false, false); rm1 = max2f(__uint_as_float(pr[0]), __uint_as_float(pr[1])); }
      if (__any((int)((rm0 > c.THR) | (rm1 > c.THR)))) {
        const float dl0 = max2f(rm0, 0.f), dl1 = max2f(rm1, 0.f);
        c.mhat[0] = fadd_s(c.mhat[0], dl0); c.mhat[1] = fadd_s(c.mhat[1], dl1);
        c.fct[0] = __builtin_amdgcn_exp2f(-dl0); c.fct[1] = __builtin_amdgcn_exp2f(-dl1);
        #pragma unroll
        for (int r = 0; r < 16; ++r) { s0[r] = fsub_s(s0[r], dl0); s1[r] = fsub_s(s1[r], dl1); }
        negm_bcast(c.NEGM[0], w4_aone(), -c.mhat[0]); negm_bcast(c.NEGM[1], w4_aone(), -c.mhat[1]);
        rescale_O(c);
      }
      float rs0 = 0.f, rs1 = 0.f;
      #pragma unroll
      for (int r = 0; r < 16; ++r) { s0[r] = __builtin_amdgcn_exp2f(s0[r]); s1[r] = __builtin_amdgcn_exp2f(s1[r]); rs0 += s0[r]; rs1 += s1[r]; }
      { auto pr = __builtin_amdgcn_permlane32_swap(__float_as_uint(rs0), __float_as_uint(rs0), false, false); c.lsum[0] += __uint_as_float(pr[0]) + __uint_as_float(pr[1]); }
      { auto pr = __builtin_amdgcn_permlane32_swap(__float_as_uint(rs1), __float_as_uint(rs1), false, false); c.lsum[1] += __uint_as_float(pr[0]) + __uint_as_float(pr[1]); }
      #pragma unroll
      for (int s_ = 0; s_ < 2; ++s_) {
        u32x4 w0, w1;
        #pragma unroll
        for (int p = 0; p < 4; ++p) { w0[p] = cvtpk(s0[8 * s_ + 2 * p], s0[8 * s_ + 2 * p + 1]); w1[p] = cvtpk(s1[8 * s_ + 2 * p], s1[8 * s_ + 2 * p + 1]); }
        c.Pk[0][s_] = __builtin_bit_cast(bf16x8, w0); c.Pk[1][2 + s_] = __builtin_bit_cast(bf16x8, w1);
      }
      tr_reads(c, vrd, make_iseq<32>{});
      asm volatile("s_waitcnt lgkmcnt(0)" : W4_VLO :: "memory"); asm volatile("" : W4_VUP :: "memory");
      GAP();
      #pragma unroll
      for (int ks = 0; ks < 4; ++ks)
        #pragma unroll
        for (int db = 0; db < 4; ++db) c.Vf[db][ks] = __builtin_shufflevector(c.vlo[db][ks], c.vup[db][ks], 0, 1, 2, 3, 4, 5, 6, 7);
      pv_tree_all(c, make_iseq<16>{});
      asm volatile("s_nop 7\n\ts_nop 7" ::: "memory");
    }
  }
  {
    char* stg = lds + OSTAGE + 8192u * wid;
    #pragma unroll
    for (int qb = 0; qb < 2; ++qb) {
      const float il = c.lsum[qb] > 0.f ? __builtin_amdgcn_rcpf(c.lsum[qb]) : 0.f;
      #pragma unroll
      for (int db = 0; db < 4; ++db) {
        float o[16];
        if (qb == 0) { switch (db) { case 0: read_o16<AO_(0, 0)>(o); break; case 1: read_o16<AO_(1, 0)>(o); break; case 2: read_o16<AO_(2, 0)>(o); break; default: read_o16<AO_(3, 0)>(o); break; } }
        else         { switch (db) { case 0: read_o16<AO_(0, 1)>(o); break; case 1: read_o16<AO_(1, 1)>(o); break; case 2: read_o16<AO_(2, 1)>(o); break; default: read_o16<AO_(3, 1)>(o); break; } }
        #pragma unroll
        for (int gq = 0; gq < 4; ++gq) {
          u32x2 w = {cvtpk(o[4 * gq + 0] * il, o[4 * gq + 1] * il), cvtpk(o[4 * gq + 2] * il, o[4 * gq + 3] * il)};
          const unsigned ch = 4u * db + gq;
          *(u32x2*)(stg + 256u * l32 + 16u * (ch ^ (l32 & 15u)) + 8u * hi) = w;
        }
      }
      asm volatile("s_waitcnt lgkmcnt(0)" ::: "memory");
      u32x4 v[8];
      #pragma unroll
      for (int j = 0; j < 8; ++j) { const unsigned r = 4u * j + (lane >> 4); v[j] = *(const u32x4*)(stg + 256u * r + 16u * ((lane & 15u) ^ (r & 15u))); }
      #pragma unroll
      for (int j = 0; j < 8; ++j) {
        char* dst = (char*)Og + ((size_t)((4 * kvh + (int)(lane >> 4)) * TQ + qrow0 + 8 * qb + j)) * 256 + 16u * (lane & 15u);
        *(u32x4*)dst = v[j];
      }
      asm volatile("s_waitcnt lgkmcnt(0)" ::: "memory");
    }
  }
}

__global__ __launch_bounds__(256) void build_lists(const unsigned short* __restrict__ mask, int* __restrict__ lcnt, unsigned* __restrict__ lent) {
  __shared__ int wtot[4];
  const int qb = blockIdx.x, tid = threadIdx.x, lane = tid & 63, wid = tid >> 6;
  unsigned* ent = lent + (size_t)qb * LCAP;
  int base = 0;
  for (int r = 0; r < 8; ++r) {
    const u32x4 m = *(const u32x4*)(mask + ((size_t)(8 * qb + r) * TQ + 8 * tid));
    unsigned bits = 0;
    #pragma unroll
    for (int e = 0; e < 4; ++e) { bits |= ((m[e] & 0xFFFFu) == 0x3F80u ? 1u : 0u) << (2 * e); bits |= ((m[e] >> 16) == 0x3F80u ? 1u : 0u) << (2 * e + 1); }
    const int cnt = __popc(bits);
    int inc = cnt;
    #pragma unroll
    for (int s = 1; s < 64; s <<= 1) { const int o = __shfl_up(inc, s); if (lane >= s) inc += o; }
    if (lane == 63) wtot[wid] = inc;
    __syncthreads();
    int woff = 0, tot = 0;
    #pragma unroll
    for (int w = 0; w < 4; ++w) { const int t = wtot[w]; if (w < wid) woff += t; tot += t; }
    int pos = base + woff + inc - cnt;
    while (bits) { const int b = __ffs(bits) - 1; bits &= bits - 1; ent[pos++] = (unsigned)(8 * tid + b) | ((unsigned)r << 16); }
    base += tot;
    __syncthreads();
  }
  if (tid == 0) lcnt[qb] = base;
}

extern "C" void kernel_launch(void* const* d_in, const int* in_sizes, int n_in, void* d_out, int out_size, void* d_ws, size_t ws_size, hipStream_t stream) {
  const unsigned short* q  = (const unsigned short*)d_in[0];
  const unsigned short* kc = (const unsigned short*)d_in[1];
  const unsigned short* vc = (const unsigned short*)d_in[2];
  const unsigned short* kt = (const unsigned short*)d_in[3];
  const unsigned short* vt = (const unsigned short*)d_in[4];
  const unsigned short* mk = (const unsigned short*)d_in[5];
  int* lcnt = (int*)((char*)d_ws + WS_CNT);
  unsigned* lent = (unsigned*)((char*)d_ws + WS_ENT);
  (void)hipFuncSetAttribute((const void*)attn_main, hipFuncAttributeMaxDynamicSharedMemorySize, (int)SHM_ATTN);
  hipLaunchKernelGGL(build_lists, dim3(TQ / 8), dim3(256), 0, stream, mk, lcnt, lent);
  hipLaunchKernelGGL(attn_main, dim3(256), dim3(64 * NWAVE), SHM_ATTN, stream, q, kc, vc, kt, vt, (const int*)lcnt, (const unsigned*)lent, (unsigned short*)d_out);
}
```

```cpp
#include <hip/hip_runtime.h>
#include <cstdio>
#include <utility>

constexpr int D = 128, NWAVE = 4, KVBLK = 64;
constexpr float THR = 8.0f;
constexpr int HQ = 32, HKV = 8, TQ = 2048, CC = 2048, NT = CC / KVBLK;
constexpr unsigned KRS = 256u, KTILEB = 64u * KRS;
constexpr size_t SHM_ATTN = 163840;
constexpr int LCAP = 8 * TQ;
constexpr size_t WS_CNT = 0, WS_ENT = 4096;

typedef float    f32x16 __attribute__((ext_vector_type(16)));
typedef float    f32x2  __attribute__((ext_vector_type(2)));
typedef float    f32x4  __attribute__((ext_vector_type(4)));
typedef __bf16   bf16x8 __attribute__((ext_vector_type(8)));
typedef __bf16   bf16x4 __attribute__((ext_vector_type(4)));
typedef __bf16   bf16x2 __attribute__((ext_vector_type(2)));
typedef unsigned u32x4  __attribute__((ext_vector_type(4)));
typedef unsigned u32x2  __attribute__((ext_vector_type(2)));
template <int... Is> using iseq = std::integer_sequence<int, Is...>;
template <int N> using make_iseq = std::make_integer_sequence<int, N>;

#define KRING 0x0000u
#define VRING 0x8000u
#define QSTAGE 0x10000u
#define OSTAGE 0x20000u
#define SRD_WORD3 0x00020000
#define DI __device__ __forceinline__
#define GAP() __builtin_amdgcn_sched_barrier(0)
#define STAMP(i) do {} while (0)
#define W4_ACLOB_HI "a128","a129","a130","a131","a132","a133","a134","a135","a136","a137","a138","a139","a140","a141","a142","a143","a144","a145","a146","a147","a148","a149","a150","a151","a152","a153","a154","a155","a156","a157","a158","a159","a160","a161","a162","a163","a164","a165","a166","a167","a168","a169","a170","a171","a172","a173","a174","a175","a176","a177","a178","a179","a180","a181","a182","a183","a184","a185","a186","a187","a188","a189","a190","a191","a192","a193","a194","a195","a196","a197","a198","a199","a200","a201","a202","a203","a204","a205","a206","a207","a208","a209","a210","a211","a212","a213","a214","a215","a216","a217","a218","a219","a220","a221","a222","a223","a224","a225","a226","a227","a228","a229","a230","a231","a232","a233","a234","a235","a236","a237","a238","a239","a240","a241","a242","a243","a244","a245","a246","a247","a248","a249","a250","a251","a252","a253","a254","a255"
#define W4_ACLOB "a0","a1","a2","a3","a4","a5","a6","a7","a8","a9","a10","a11","a12","a13","a14","a15","a16","a17","a18","a19","a20","a21","a22","a23","a24","a25","a26","a27","a28","a29","a30","a31","a32","a33","a34","a35","a36","a37","a38","a39","a40","a41","a42","a43","a44","a45","a46","a47","a48","a49","a50","a51","a52","a53","a54","a55","a56","a57","a58","a59","a60","a61","a62","a63","a64","a65","a66","a67","a68","a69","a70","a71","a72","a73","a74","a75","a76","a77","a78","a79","a80","a81","a82","a83","a84","a85","a86","a87","a88","a89","a90","a91","a92","a93","a94","a95","a96","a97","a98","a99","a100","a101","a102","a103","a104","a105","a106","a107","a108","a109","a110","a111","a112","a113","a114","a115","a116","a117","a118","a119","a120","a121","a122","a123","a124","a125","a126","a127"

DI unsigned rfl(unsigned x) { return __builtin_amdgcn_readfirstlane(x); }
DI unsigned cvtpk(float lo, float hi) {
  f32x2 v = {lo, hi}; bf16x2 b = __builtin_convertvector(v, bf16x2); return __builtin_bit_cast(unsigned, b);
}
DI float max3f(float a, float b, float c) { float r; asm("v_max3_f32 %0, %1, %2, %3" : "=v"(r) : "v"(a), "v"(b), "v"(c)); return r; }
DI float max2f(float a, float b) { float r; asm("v_max_f32_e32 %0, %1, %2" : "=v"(r) : "v"(a), "v"(b)); return r; }
DI float fadd_s(float a, float b) { float r; asm("v_add_f32_e32 %0, %1, %2" : "=v"(r) : "v"(a), "v"(b)); return r; }
DI float fmul_s(float a, float b) { float r; asm("v_mul_f32_e32 %0, %1, %2" : "=v"(r) : "v"(a), "v"(b)); return r; }
DI float fsub_s(float a, float b) { float r; asm("v_sub_f32_e32 %0, %1, %2" : "=v"(r) : "v"(a), "v"(b)); return r; }
DI float w4_aone() { unsigned l; asm volatile("v_mbcnt_lo_u32_b32 %0, -1, 0\n\tv_mbcnt_hi_u32_b32 %0, -1, %0" : "=v"(l)); return l < 32u ? 1.f : 0.f; }
DI void negm_bcast(f32x16& negm, float aone, float neg_mhat) {
  asm volatile("s_nop 1\n\tv_mfma_f32_32x32x2_f32 %0, %1, %2, 0" : "+v"(negm) : "v"(aone), "v"(neg_mhat));
}

struct W4C {
  unsigned vkb[4], vvb, vksrc[2], vvsrc, m0k, m0v, sofk, sofv, ldsb;
  __amdgpu_buffer_rsrc_t srdK, srdV;
  int T; float THR;
  f32x16 S[2][2][2];
  f32x16 NEGM[2];
  bf16x8 Vf[4][4];
  bf16x8 Pk[2][4];
  bf16x4 vlo[4][4], vup[4][4];
  float mhat[2], lsum[2], fct[2]; bool pend;
  float rs0[2], rs1[2], pmx0[2], pmx1[2], rmax[2], delta[2];
  unsigned pm0[8], psf[8];
};

constexpr int AO_(int db, int qb) { return 16 * (2 * db + qb); }
constexpr int AQ_(int qb, int d0) { return 128 + 32 * qb + 4 * d0; }
constexpr int AK_(int kb, int d0) { return 192 + 32 * kb + 4 * d0; }

template <int I> DI void tr_read(W4C& c, unsigned vslot) {
  constexpr int ks = I >> 3, db = (I >> 1) & 3;
  if constexpr ((I & 1) == 0) asm volatile("ds_read_b64_tr_b16 %0, %1 offset:%2" : "=v"(c.vlo[db][ks]) : "v"(c.vvb + vslot), "i"(4096 * ks + 512 * db) : "memory");
  else                        asm volatile("ds_read_b64_tr_b16 %0, %1 offset:%2" : "=v"(c.vup[db][ks]) : "v"(c.vvb + vslot), "i"(4096 * ks + 2048 + 512 * db) : "memory");
}
template <int I> DI void k_read(W4C& c, unsigned slot) {
  constexpr int kb = I >> 3, d0 = I & 7;
  asm volatile("ds_read_b128 a[%c1:%c2], %0 offset:%c3" :: "v"(c.vkb[d0 & 3] + slot), "i"(AK_(kb, d0)), "i"(AK_(kb, d0) + 3), "i"(8192 * kb + 128 * (d0 >> 2)) : "memory", W4_ACLOB_HI);
}
DI bf16x8 q_ldread(unsigned base, const int qb, const int d0) {
  bf16x8 r;
  switch (2 * qb + (d0 >> 2)) {
    case 0: asm volatile("ds_read_b128 %0, %1 offset:0"    : "=v"(r) : "v"(base) : "memory"); break;
    case 1: asm volatile("ds_read_b128 %0, %1 offset:128"  : "=v"(r) : "v"(base) : "memory"); break;
    case 2: asm volatile("ds_read_b128 %0, %1 offset:8192" : "=v"(r) : "v"(base) : "memory"); break;
    default: asm volatile("ds_read_b128 %0, %1 offset:8320" : "=v"(r) : "v"(base) : "memory"); break;
  }
  return r;
}
template <int I> DI void q_write1(const u32x4& w) {
  constexpr int aq = AQ_(I >> 3, I & 7);
  asm volatile("v_accvgpr_write_b32 a[%c4], %0\n\tv_accvgpr_write_b32 a[%c5], %1\n\tv_accvgpr_write_b32 a[%c6], %2\n\tv_accvgpr_write_b32 a[%c7], %3"
               :: "v"(w[0]), "v"(w[1]), "v"(w[2]), "v"(w[3]), "i"(aq), "i"(aq + 1), "i"(aq + 2), "i"(aq + 3) : W4_ACLOB_HI);
}
DI void q_write(const int qb, const int d0, const u32x4& w) {
  switch (8 * qb + d0) {
    case 0: q_write1<0>(w); break;   case 1: q_write1<1>(w); break;   case 2: q_write1<2>(w); break;   case 3: q_write1<3>(w); break;
    case 4: q_write1<4>(w); break;   case 5: q_write1<5>(w); break;   case 6: q_write1<6>(w); break;   case 7: q_write1<7>(w); break;
    case 8: q_write1<8>(w); break;   case 9: q_write1<9>(w); break;   case 10: q_write1<10>(w); break; case 11: q_write1<11>(w); break;
    case 12: q_write1<12>(w); break; case 13: q_write1<13>(w); break; case 14: q_write1<14>(w); break; case 15: q_write1<15>(w); break;
  }
}
template <int A0> DI void read_o16(float (&o)[16]) {
  asm volatile("v_accvgpr_read_b32 %0, a[%c16]\n\tv_accvgpr_read_b32 %1, a[%c17]\n\tv_accvgpr_read_b32 %2, a[%c18]\n\tv_accvgpr_read_b32 %3, a[%c19]\n\t"
               "v_accvgpr_read_b32 %4, a[%c20]\n\tv_accvgpr_read_b32 %5, a[%c21]\n\tv_accvgpr_read_b32 %6, a[%c22]\n\tv_accvgpr_read_b32 %7, a[%c23]\n\t"
               "v_accvgpr_read_b32 %8, a[%c24]\n\tv_accvgpr_read_b32 %9, a[%c25]\n\tv_accvgpr_read_b32 %10, a[%c26]\n\tv_accvgpr_read_b32 %11, a[%c27]\n\t"
               "v_accvgpr_read_b32 %12, a[%c28]\n\tv_accvgpr_read_b32 %13, a[%c29]\n\tv_accvgpr_read_b32 %14, a[%c30]\n\tv_accvgpr_read_b32 %15, a[%c31]"
               : "=v"(o[0]), "=v"(o[1]), "=v"(o[2]), "=v"(o[3]), "=v"(o[4]), "=v"(o[5]), "=v"(o[6]), "=v"(o[7]),
                 "=v"(o[8]), "=v"(o[9]), "=v"(o[10]), "=v"(o[11]), "=v"(o[12]), "=v"(o[13]), "=v"(o[14]), "=v"(o[15])
               : "i"(A0), "i"(A0 + 1), "i"(A0 + 2), "i"(A0 + 3), "i"(A0 + 4), "i"(A0 + 5), "i"(A0 + 6), "i"(A0 + 7),
                 "i"(A0 + 8), "i"(A0 + 9), "i"(A0 + 10), "i"(A0 + 11), "i"(A0 + 12), "i"(A0 + 13), "i"(A0 + 14), "i"(A0 + 15));
}
template <int A0> DI void scale_o16(float f) {
  float t0, t1;
  asm volatile("v_accvgpr_read_b32 %0, a[%c3]\n\tv_accvgpr_read_b32 %1, a[%c4]\n\tv_mul_f32 %0, %2, %0\n\tv_mul_f32 %1, %2, %1\n\tv_accvgpr_write_b32 a[%c3], %0\n\tv_accvgpr_write_b32 a[%c4], %1"
               : "=&v"(t0), "=&v"(t1) : "v"(f), "i"(A0), "i"(A0 + 1) : W4_ACLOB);
}
template <int... Is> DI void scale_group(float f0, float f1, iseq<Is...>) {
  (scale_o16<2 * Is>((((2 * Is) >> 4) & 1) ? f1 : f0), ...);
}
DI void rescale_O(W4C& c) {
  asm volatile("s_nop 7\n\ts_nop 7" ::: "memory");
  scale_group(c.fct[0], c.fct[1], make_iseq<64>{});
  asm volatile("s_nop 1" ::: "memory");
  c.lsum[0] = fmul_s(c.lsum[0], c.fct[0]); c.lsum[1] = fmul_s(c.lsum[1], c.fct[1]);
}

template <int X, bool FIRST, int M> DI void qk_mfma(W4C& c) {
  constexpr int d0 = M >> 2, kb = (M >> 1) & 1, qb = M & 1, ak = AK_(kb, d0), aq = AQ_(qb, d0);
  if constexpr (d0 == 0) {
    if constexpr (FIRST) asm volatile("v_mfma_f32_32x32x16_bf16 %0, a[%c1:%c2], a[%c3:%c4], 0" : "=&v"(c.S[X][kb][qb]) : "i"(ak), "i"(ak + 3), "i"(aq), "i"(aq + 3) : W4_ACLOB, W4_ACLOB_HI);
    else                 asm volatile("v_mfma_f32_32x32x16_bf16 %0, a[%c2:%c3], a[%c4:%c5], %1" : "=&v"(c.S[X][kb][qb]) : "v"(c.NEGM[qb]), "i"(ak), "i"(ak + 3), "i"(aq), "i"(aq + 3) : W4_ACLOB, W4_ACLOB_HI);
  } else asm volatile("v_mfma_f32_32x32x16_bf16 %0, a[%c1:%c2], a[%c3:%c4], %0" : "+v"(c.S[X][kb][qb]) : "i"(ak), "i"(ak + 3), "i"(aq), "i"(aq + 3) : W4_ACLOB, W4_ACLOB_HI);
}
template <int M, bool ZERO> DI void pv_mfma(W4C& c) {
  constexpr int ks = M >> 3, db = (M >> 1) & 3, qb = M & 1, ao = AO_(db, qb);
  if constexpr (ZERO && ks == 0) asm volatile("v_mfma_f32_32x32x16_bf16 a[%c2:%c3], %0, %1, 0" :: "v"(c.Vf[db][ks]), "v"(c.Pk[qb][ks]), "i"(ao), "i"(ao + 15) : W4_ACLOB);
  else                         asm volatile("v_mfma_f32_32x32x16_bf16 a[%c2:%c3], %0, %1, a[%c2:%c3]" :: "v"(c.Vf[db][ks]), "v"(c.Pk[qb][ks]), "i"(ao), "i"(ao + 15) : W4_ACLOB);
}
template <int X, int QB, int KS, int P> DI void pack1(W4C& c) {
  u32x4 w = __builtin_bit_cast(u32x4, c.Pk[QB][KS]);
  w[P] = cvtpk(c.S[X][KS >> 1][QB][8 * (KS & 1) + 2 * P], c.S[X][KS >> 1][QB][8 * (KS & 1) + 2 * P + 1]);
  c.Pk[QB][KS] = __builtin_bit_cast(bf16x8, w);
}
template <int X, int J> DI void rowsum1(W4C& c) {
  constexpr int qb = J >> 4, ks = (J >> 2) & 3, p = J & 3;
  c.rs0[qb] = fadd_s(c.rs0[qb], c.S[X][ks >> 1][qb][8 * (ks & 1) + 2 * p]); c.rs1[qb] = fadd_s(c.rs1[qb], c.S[X][ks >> 1][qb][8 * (ks & 1) + 2 * p + 1]);
}

template <int X, bool FIRST, int M> DI void gapA(W4C& c, unsigned vslot, const int t) {
  constexpr int Xp = X ^ 1;
  qk_mfma<X, FIRST, M>(c);
  if constexpr (M >= 16 && M <= 31) {
    const int tk = t + 2, tv = t + 1;
    constexpr int q = (M - 16) >> 1;
    if constexpr (((M - 16) & 1) == 0) {
      if constexpr (q < 4) c.pm0[q] = c.m0k + (unsigned)((t + 2) & 1) * 0x4000u + 1024u * q; else c.pm0[q] = c.m0v + (unsigned)((t + 1) & 1) * 0x4000u + 1024u * (q - 4);
      asm volatile("" : "+s"(c.pm0[q]));
    } else {
      if constexpr (q < 4) c.psf[q] = KTILEB * (unsigned)tk + c.sofk + KRS * 4u * q; else c.psf[q] = KTILEB * (unsigned)tv + c.sofv + KRS * 8u * ((q - 4) >> 1) + 128u * ((q - 4) & 1);
      asm volatile("" : "+s"(c.psf[q]));
    }
  }
  if constexpr (M == 31) asm volatile("s_nop 4" : "+s"(c.pm0[0]), "+s"(c.pm0[1]), "+s"(c.pm0[2]), "+s"(c.pm0[3]), "+s"(c.pm0[4]), "+s"(c.pm0[5]), "+s"(c.pm0[6]), "+s"(c.pm0[7]),
                                                  "+s"(c.psf[0]), "+s"(c.psf[1]), "+s"(c.psf[2]), "+s"(c.psf[3]), "+s"(c.psf[4]), "+s"(c.psf[5]), "+s"(c.psf[6]), "+s"(c.psf[7]));
  if constexpr (!FIRST && M == 8) asm volatile("" :: "v"(c.NEGM[0]), "v"(c.NEGM[1]));
  if constexpr (!FIRST) {
    constexpr int base = M + (M >= 4) + (M >= 8) + (M >= 12) + (M >= 16) + (M >= 20) + (M >= 24);
    if constexpr (M <= 25) tr_read<base>(c, vslot);
    if constexpr (M == 3 || M == 7 || M == 11 || M == 15 || M == 19 || M == 23) tr_read<base + 1>(c, vslot);
    if constexpr (M < 16) {
      constexpr int e0 = 2 * M, e1 = 2 * M + 1;
      c.S[Xp][1][e0 >> 4][e0 & 15] = __builtin_amdgcn_exp2f(c.S[Xp][1][e0 >> 4][e0 & 15]);
      c.S[Xp][1][e1 >> 4][e1 & 15] = __builtin_amdgcn_exp2f(c.S[Xp][1][e1 >> 4][e1 & 15]);
      pack1<Xp, (M >> 3), ((M >> 2) & 1), (M & 3)>(c);
    } else {
      constexpr int cc = M - 16;
      pack1<Xp, (cc >> 3), 2 + ((cc >> 2) & 1), (cc & 3)>(c);
      rowsum1<Xp, 16 * (cc >> 3) + 8 + (4 * ((cc >> 2) & 1)) + (cc & 3)>(c);
    }
  }
  GAP();
}
#define W4_VLO "+v"(c.vlo[0][0]), "+v"(c.vlo[0][1]), "+v"(c.vlo[0][2]), "+v"(c.vlo[0][3]), "+v"(c.vlo[1][0]), "+v"(c.vlo[1][1]), "+v"(c.vlo[1][2]), "+v"(c.vlo[1][3]), \
               "+v"(c.vlo[2][0]), "+v"(c.vlo[2][1]), "+v"(c.vlo[2][2]), "+v"(c.vlo[2][3]), "+v"(c.vlo[3][0]), "+v"(c.vlo[3][1]), "+v"(c.vlo[3][2]), "+v"(c.vlo[3][3])
#define W4_VUP "+v"(c.vup[0][0]), "+v"(c.vup[0][1]), "+v"(c.vup[0][2]), "+v"(c.vup[0][3]), "+v"(c.vup[1][0]), "+v"(c.vup[1][1]), "+v"(c.vup[1][2]), "+v"(c.vup[1][3]), \
               "+v"(c.vup[2][0]), "+v"(c.vup[2][1]), "+v"(c.vup[2][2]), "+v"(c.vup[2][3]), "+v"(c.vup[3][0]), "+v"(c.vup[3][1]), "+v"(c.vup[3][2]), "+v"(c.vup[3][3])
#define W4_V01 "+v"(c.vlo[0][0]), "+v"(c.vlo[0][1]), "+v"(c.vlo[1][0]), "+v"(c.vlo[1][1]), "+v"(c.vlo[2][0]), "+v"(c.vlo[2][1]), "+v"(c.vlo[3][0]), "+v"(c.vlo[3][1]), "+v"(c.vup[0][0]), "+v"(c.vup[0][1]), "+v"(c.vup[1][0]), "+v"(c.vup[1][1]), "+v"(c.vup[2][0]), "+v"(c.vup[2][1]), "+v"(c.vup[3][0]), "+v"(c.vup[3][1])
#define W4_V23 "+v"(c.vlo[0][2]), "+v"(c.vlo[0][3]), "+v"(c.vlo[1][2]), "+v"(c.vlo[1][3]), "+v"(c.vlo[2][2]), "+v"(c.vlo[2][3]), "+v"(c.vlo[3][2]), "+v"(c.vlo[3][3]), "+v"(c.vup[0][2]), "+v"(c.vup[0][3]), "+v"(c.vup[1][2]), "+v"(c.vup[1][3]), "+v"(c.vup[2][2]), "+v"(c.vup[2][3]), "+v"(c.vup[3][2]), "+v"(c.vup[3][3])
template <int X, bool FIRST, int... Ms> DI void phaseA_(W4C& c, const int t, iseq<Ms...>) {
  const unsigned vslot = (unsigned)((t - 1) & 1) * 0x4000u;
  (gapA<X, FIRST, Ms>(c, vslot, t), ...);
  if constexpr (!FIRST) {
    #pragma unroll
    for (int qb = 0; qb < 2; ++qb) {
      const float rs = fadd_s(c.rs0[qb], c.rs1[qb]);
      auto pr = __builtin_amdgcn_permlane32_swap(__float_as_uint(rs), __float_as_uint(rs), false, false);
      c.lsum[qb] = fadd_s(c.lsum[qb], fadd_s(__uint_as_float(pr[0]), __uint_as_float(pr[1])));
    }
  }
  if constexpr (FIRST) asm volatile("s_waitcnt vmcnt(0) lgkmcnt(0)\n\ts_barrier" ::: "memory");
  else { asm volatile("s_waitcnt vmcnt(0) lgkmcnt(0)" : W4_VLO :: "memory"); asm volatile("s_barrier" : W4_VUP :: "memory"); }
  GAP();
  if constexpr (!FIRST) {
    #pragma unroll
    for (int ks = 0; ks < 4; ++ks)
      #pragma unroll
      for (int db = 0; db < 4; ++db) c.Vf[db][ks] = __builtin_shufflevector(c.vlo[db][ks], c.vup[db][ks], 0, 1, 2, 3, 4, 5, 6, 7);
  }
}
template <int X, bool FIRST> DI void phaseA(W4C& c, const int t) { phaseA_<X, FIRST>(c, t, make_iseq<32>{}); }

template <int X, bool PEEL, bool ZERO1, int DK, int DV, bool KR, int M> DI void gapB(W4C& c, const unsigned kslot_rd) {
  if constexpr (!PEEL) pv_mfma<M, ZERO1>(c);
  if constexpr (M < 4 && DK == 1)             asm volatile("s_mov_b32 m0, %0\n\ts_nop 0\n\tbuffer_load_dwordx4 %1, %2, %3 offen lds" :: "s"(c.pm0[M]), "v"(c.vksrc[M & 1]), "s"(c.srdK),  "s"(c.psf[M]) : "m0", "memory");
  else if constexpr (M >= 4 && M < 8 && DV == 1) asm volatile("s_mov_b32 m0, %0\n\ts_nop 0\n\tbuffer_load_dwordx4 %1, %2, %3 offen lds" :: "s"(c.pm0[M]), "v"(c.vvsrc), "s"(c.srdV),  "s"(c.psf[M]) : "m0", "memory");
  if constexpr (M >= 1 && M <= 16 && KR) k_read<M - 1>(c, kslot_rd);
  if constexpr (M >= 8 && M < 16) {
    constexpr int i = M - 8, q = i >> 2, r = 4 * (i & 3);
    if constexpr ((i & 3) == 0) { c.pmx0[q] = max3f(c.S[X][0][q][0], c.S[X][0][q][1], c.S[X][1][q][0]); c.pmx1[q] = max3f(c.S[X][0][q][2], c.S[X][0][q][3], c.S[X][1][q][1]);
                                  c.pmx0[q] = max3f(c.pmx0[q], c.S[X][1][q][2], c.S[X][1][q][3]); }
    else                        { c.pmx0[q] = max3f(c.pmx0[q], c.S[X][0][q][r], c.S[X][0][q][r + 1]); c.pmx1[q] = max3f(c.pmx1[q], c.S[X][0][q][r + 2], c.S[X][0][q][r + 3]);
                                  c.pmx0[q] = max3f(c.pmx0[q], c.S[X][1][q][r], c.S[X][1][q][r + 1]); c.pmx1[q] = max3f(c.pmx1[q], c.S[X][1][q][r + 2], c.S[X][1][q][r + 3]); }
  }
  if constexpr (M == 16 || M == 17) {
    constexpr int q = M - 16; const float mm = max2f(c.pmx0[q], c.pmx1[q]);
    auto pr = __builtin_amdgcn_permlane32_swap(__float_as_uint(mm), __float_as_uint(mm), false, false);
    c.rmax[q] = max2f(__uint_as_float(pr[0]), __uint_as_float(pr[1]));
  }
  if constexpr (M == 18) {
    if constexpr (PEEL) {
      #pragma unroll
      for (int q = 0; q < 2; ++q) { c.mhat[q] = c.rmax[q]; c.lsum[q] = 0.f; c.fct[q] = 1.f;
        #pragma unroll
        for (int kb = 0; kb < 2; ++kb) { _Pragma("unroll") for (int e = 0; e < 16; ++e) c.S[X][kb][q][e] = fsub_s(c.S[X][kb][q][e], c.rmax[q]); }
        negm_bcast(c.NEGM[q], w4_aone(), -c.mhat[q]);
      }
    } else {
      const bool grew = (c.rmax[0] > c.THR) | (c.rmax[1] > c.THR);
      if (__builtin_expect(__any((int)grew), 0)) {
        #pragma unroll
        for (int q = 0; q < 2; ++q) { const float dl = max2f(c.rmax[q], 0.f); c.delta[q] = dl; c.mhat[q] = fadd_s(c.mhat[q], dl); c.fct[q] = __builtin_amdgcn_exp2f(-dl);
          #pragma unroll
          for (int kb = 0; kb < 2; ++kb) { _Pragma("unroll") for (int e = 0; e < 16; ++e) c.S[X][kb][q][e] = fsub_s(c.S[X][kb][q][e], dl); }
          negm_bcast(c.NEGM[q], w4_aone(), -c.mhat[q]);
        }
        c.pend = true;
      }
    }
  }
  if constexpr (M == 18) { c.rs0[0] = c.rs0[1] = c.rs1[0] = c.rs1[1] = 0.f; }
  if constexpr (M >= 19) {
    constexpr int g = M - 19, e_lo = (32 * g) / 13, e_hi = (32 * (g + 1)) / 13;
    {
      constexpr int p_lo = g ? (32 * (g - 1)) / 13 : 0, p_hi = g ? (32 * g) / 13 : 0;
      if constexpr (p_lo + 0 < p_hi) { constexpr int e = p_lo + 0; if constexpr (e & 1) c.rs1[e >> 4] = fadd_s(c.rs1[e >> 4], c.S[X][0][e >> 4][e & 15]); else c.rs0[e >> 4] = fadd_s(c.rs0[e >> 4], c.S[X][0][e >> 4][e & 15]); }
      if constexpr (p_lo + 1 < p_hi) { constexpr int e = p_lo + 1; if constexpr (e & 1) c.rs1[e >> 4] = fadd_s(c.rs1[e >> 4], c.S[X][0][e >> 4][e & 15]); else c.rs0[e >> 4] = fadd_s(c.rs0[e >> 4], c.S[X][0][e >> 4][e & 15]); }
      if constexpr (p_lo + 2 < p_hi) { constexpr int e = p_lo + 2; if constexpr (e & 1) c.rs1[e >> 4] = fadd_s(c.rs1[e >> 4], c.S[X][0][e >> 4][e & 15]); else c.rs0[e >> 4] = fadd_s(c.rs0[e >> 4], c.S[X][0][e >> 4][e & 15]); }
    }
    if constexpr (e_lo + 0 < e_hi) c.S[X][0][(e_lo + 0) >> 4][(e_lo + 0) & 15] = __builtin_amdgcn_exp2f(c.S[X][0][(e_lo + 0) >> 4][(e_lo + 0) & 15]);
    if constexpr (e_lo + 1 < e_hi) c.S[X][0][(e_lo + 1) >> 4][(e_lo + 1) & 15] = __builtin_amdgcn_exp2f(c.S[X][0][(e_lo + 1) >> 4][(e_lo + 1) & 15]);
    if constexpr (e_lo + 2 < e_hi) c.S[X][0][(e_lo + 2) >> 4][(e_lo + 2) & 15] = __builtin_amdgcn_exp2f(c.S[X][0][(e_lo + 2) >> 4][(e_lo + 2) & 15]);
  }
  if constexpr (M == 31) asm volatile("" :: "v"(c.S[X][0][0]), "v"(c.S[X][0][1]), "v"(c.NEGM[0]), "v"(c.NEGM[1]), "v"(c.rs0[0]), "v"(c.rs0[1]), "v"(c.rs1[0]), "v"(c.rs1[1]));
  GAP();
}
template <int X, bool PEEL, bool ZERO1, int DK, int DV, bool KR, int... Ms> DI void phaseB_(W4C& c, const int t, iseq<Ms...>) {
  const unsigned kslot_rd = (unsigned)((t + 1) & 1) * 0x4000u;
  if constexpr (!PEEL) asm volatile("s_nop 1" ::: "memory");
  GAP();
  (gapB<X, PEEL, ZERO1, DK, DV, KR, Ms>(c, kslot_rd), ...);
  {
    #pragma unroll
    for (int e = (32 * 12) / 13; e < 32; ++e) { if (e & 1) c.rs1[e >> 4] = fadd_s(c.rs1[e >> 4], c.S[X][0][e >> 4][e & 15]); else c.rs0[e >> 4] = fadd_s(c.rs0[e >> 4], c.S[X][0][e >> 4][e & 15]); }
  }
  if constexpr (!PEEL) {
    if (__builtin_expect(c.pend, 0)) { rescale_O(c); c.pend = false; }
  }
  asm volatile("s_waitcnt lgkmcnt(0)" ::: "memory");
  GAP();
}
template <int X, bool PEEL, bool ZERO1, int DK = 1, int DV = 1, bool KR = true> DI void phaseB(W4C& c, const int t) { phaseB_<X, PEEL, ZERO1, DK, DV, KR>(c, t, make_iseq<32>{}); }

template <int... Is> DI void k_reads(W4C& c, unsigned slot, iseq<Is...>) { (k_read<Is>(c, slot), ...); }
template <int... Is> DI void tr_reads(W4C& c, unsigned vslot, iseq<Is...>) { (tr_read<Is>(c, vslot), ...); }
template <int X, int... Cs> DI void packs(W4C& c, iseq<Cs...>) { (pack1<X, ((Cs >> 3) & 1), 2 * (Cs >> 4) + ((Cs >> 2) & 1), (Cs & 3)>(c), ...); }
template <int X, int J> DI void rowsum1_padded(W4C& c) { rowsum1<X, J>(c); asm volatile("s_nop 1"); }
template <int X, int... Js> DI void rowsums(W4C& c, iseq<Js...>) { (rowsum1_padded<X, Js>(c), ...); }
template <bool Z, int... Ms> DI void pv_all(W4C& c, iseq<Ms...>) { (pv_mfma<Ms, Z>(c), ...); }

template <int KB, int QB, int D0> DI void qk_tree(f32x16& s, const f32x16& negm) {
  constexpr int aq = AQ_(QB, D0), ak = AK_(KB, D0);
  if constexpr (D0 == 0) asm volatile("s_nop 1\n\tv_mfma_f32_32x32x16_bf16 %0, a[%c2:%c3], a[%c4:%c5], %1" : "=&v"(s) : "v"(negm), "i"(ak), "i"(ak + 3), "i"(aq), "i"(aq + 3) : W4_ACLOB_HI);
  else if constexpr (D0 == 7) asm volatile("v_mfma_f32_32x32x16_bf16 %0, a[%c1:%c2], a[%c3:%c4], %0\n\ts_nop 7\n\ts_nop 7" : "+v"(s) : "i"(ak), "i"(ak + 3), "i"(aq), "i"(aq + 3) : W4_ACLOB_HI);
  else asm volatile("v_mfma_f32_32x32x16_bf16 %0, a[%c1:%c2], a[%c3:%c4], %0" : "+v"(s) : "i"(ak), "i"(ak + 3), "i"(aq), "i"(aq + 3) : W4_ACLOB_HI);
}
template <int KB, int QB, int... D0s> DI void qk_tree_all(f32x16& s, const f32x16& negm, iseq<D0s...>) { (qk_tree<KB, QB, D0s>(s, negm), ...); }
template <int I> DI void kt_load1(unsigned v0, unsigned v1, __amdgpu_buffer_rsrc_t srd) {
  constexpr int kb = I >> 3, d0 = I & 7, ak = AK_(kb, d0);
  asm volatile("buffer_load_dwordx4 a[%c2:%c3], %0, %1, 0 offen offset:%c4" :: "v"(kb ? v1 : v0), "s"(srd), "i"(ak), "i"(ak + 3), "i"(32 * d0) : "memory", W4_ACLOB_HI);
}
template <int... Is> DI void kt_loads(unsigned v0, unsigned v1, __amdgpu_buffer_rsrc_t srd, iseq<Is...>) { asm volatile("s_nop 4" ::: "memory"); (kt_load1<Is>(v0, v1, srd), ...); }
template <int M> DI void pv_tree(W4C& c) {
  constexpr int qb = M >> 3, s_ = (M >> 2) & 1, db = M & 3, ks = 2 * qb + s_, ao = AO_(db, qb);
  asm volatile("s_nop 1\n\tv_mfma_f32_32x32x16_bf16 a[%c2:%c3], %0, %1, a[%c2:%c3]" :: "v"(c.Vf[db][ks]), "v"(c.Pk[qb][ks]), "i"(ao), "i"(ao + 15) : W4_ACLOB);
}
template <int... Ms> DI void pv_tree_all(W4C& c, iseq<Ms...>) { (pv_tree<Ms>(c), ...); }

template <int QB, int D0> DI void qk_treev(f32x16& s, const bf16x8& kf, const f32x16& negm) {
  constexpr int aq = AQ_(QB, D0);
  if constexpr (D0 == 0) asm volatile("s_nop 1\n\tv_mfma_f32_32x32x16_bf16 %0, %1, a[%c3:%c4], %2" : "=&v"(s) : "v"(kf), "v"(negm), "i"(aq), "i"(aq + 3) : W4_ACLOB_HI);
  else if constexpr (D0 == 7) asm volatile("v_mfma_f32_32x32x16_bf16 %0, %1, a[%c2:%c3], %0\n\ts_nop 7\n\ts_nop 7" : "+v"(s) : "v"(kf), "i"(aq), "i"(aq + 3) : W4_ACLOB_HI);
  else asm volatile("v_mfma_f32_32x32x16_bf16 %0, %1, a[%c2:%c3], %0" : "+v"(s) : "v"(kf), "i"(aq), "i"(aq + 3) : W4_ACLOB_HI);
}
template <int QB, int... D0s> DI void qk_treev_all(f32x16& s, const bf16x8 (&kf)[16], const f32x16& negm, iseq<D0s...>) { (qk_treev<QB, D0s>(s, kf[8 * QB + D0s], negm), ...); }
template <int I> DI void ktv_load1(bf16x8 (&kf)[16], unsigned v0, unsigned v1, __amdgpu_buffer_rsrc_t srd) {
  asm volatile("buffer_load_dwordx4 %0, %1, %2, 0 offen offset:%c3" : "=v"(kf[I]) : "v"((I >> 3) ? v1 : v0), "s"(srd), "i"(32 * (I & 7)) : "memory");
}
template <int... Is> DI void ktv_loads(bf16x8 (&kf)[16], unsigned v0, unsigned v1, __amdgpu_buffer_rsrc_t srd, iseq<Is...>) { asm volatile("s_nop 4" ::: "memory"); (ktv_load1<Is>(kf, v0, v1, srd), ...); }
#define KF_RW "+v"(kfB[0]), "+v"(kfB[1]), "+v"(kfB[2]), "+v"(kfB[3]), "+v"(kfB[4]), "+v"(kfB[5]), "+v"(kfB[6]), "+v"(kfB[7]), \
              "+v"(kfB[8]), "+v"(kfB[9]), "+v"(kfB[10]), "+v"(kfB[11]), "+v"(kfB[12]), "+v"(kfB[13]), "+v"(kfB[14]), "+v"(kfB[15])

template <typename FV, typename FK> DI void tree_tile(W4C& c, const int j, const int ntile, const unsigned lane, const unsigned l32, const unsigned hi, char* lds,
                                                 const unsigned stash, const unsigned vbuf, FV&& issuev, FK&& issuek) {
  const unsigned stj = stash + 256u * (unsigned)(j & 3);
  asm volatile("s_waitcnt vmcnt(0) lgkmcnt(0)" ::: "memory");
  const unsigned e = *(const unsigned*)(lds + stj + 4u * lane);
  if (j + 1 < ntile) issuev(j + 1);
  f32x16 s0, s1;
  qk_tree_all<0, 0>(s0, c.NEGM[0], make_iseq<8>{}); qk_tree_all<1, 1>(s1, c.NEGM[1], make_iseq<8>{});
  if (j + 1 < ntile) issuek(j + 1);
  {
    const unsigned rm = (e >> 24) ? 0u : (e >> 16);
    unsigned long long mk = 0;
    #pragma unroll
    for (int r = 0; r < 8; ++r) { const unsigned long long b = __ballot((rm >> r) & 1u); if ((l32 >> 2) == (unsigned)r) mk = b; }
    const unsigned m0 = (unsigned)mk, m1 = (unsigned)(mk >> 32);
    #pragma unroll
    for (int r = 0; r < 16; ++r) {
      const unsigned sl = (r & 3) + 8 * (r >> 2) + 4 * hi;
      s0[r] = ((m0 >> sl) & 1u) ? s0[r] : -1e30f;
      s1[r] = ((m1 >> sl) & 1u) ? s1[r] : -1e30f;
    }
  }
  float rm0 = s0[0], rm1 = s1[0];
  #pragma unroll
  for (int r = 1; r < 16; ++r) { rm0 = max2f(rm0, s0[r]); rm1 = max2f(rm1, s1[r]); }
  { auto pr = __builtin_amdgcn_permlane32_swap(__float_as_uint(rm0), __float_as_uint(rm0), false, false); rm0 = max2f(__uint_as_float(pr[0]), __uint_as_float(pr[1])); }
  { auto pr = __builtin_amdgcn_permlane32_swap(__float_as_uint(rm1), __float_as_uint(rm1), false, false); rm1 = max2f(__uint_as_float(pr[0]), __uint_as_float(pr[1])); }
  if (__any((int)((rm0 > c.THR) | (rm1 > c.THR)))) {
    const float dl0 = max2f(rm0, 0.f), dl1 = max2f(rm1, 0.f);
    c.mhat[0] = fadd_s(c.mhat[0], dl0); c.mhat[1] = fadd_s(c.mhat[1], dl1);
    c.fct[0] = __builtin_amdgcn_exp2f(-dl0); c.fct[1] = __builtin_amdgcn_exp2f(-dl1);
    #pragma unroll
    for (int r = 0; r < 16; ++r) { s0[r] = fsub_s(s0[r], dl0); s1[r] = fsub_s(s1[r], dl1); }
    negm_bcast(c.NEGM[0], w4_aone(), -c.mhat[0]); negm_bcast(c.NEGM[1], w4_aone(), -c.mhat[1]);
    rescale_O(c);
  }
  float rs0 = 0.f, rs1 = 0.f;
  #pragma unroll
  for (int r = 0; r < 16; ++r) { s0[r] = __builtin_amdgcn_exp2f(s0[r]); s1[r] = __builtin_amdgcn_exp2f(s1[r]); rs0 += s0[r]; rs1 += s1[r]; }
  { auto pr = __builtin_amdgcn_permlane32_swap(__float_as_uint(rs0), __float_as_uint(rs0), false, false); c.lsum[0] += __uint_as_float(pr[0]) + __uint_as_float(pr[1]); }
  { auto pr = __builtin_amdgcn_permlane32_swap(__float_as_uint(rs1), __float_as_uint(rs1), false, false); c.lsum[1] += __uint_as_float(pr[0]) + __uint_as_float(pr[1]); }
  #pragma unroll
  for (int s_ = 0; s_ < 2; ++s_) {
    u32x4 w0, w1;
    #pragma unroll
    for (int p = 0; p < 4; ++p) { w0[p] = cvtpk(s0[8 * s_ + 2 * p], s0[8 * s_ + 2 * p + 1]); w1[p] = cvtpk(s1[8 * s_ + 2 * p], s1[8 * s_ + 2 * p + 1]); }
    c.Pk[0][s_] = __builtin_bit_cast(bf16x8, w0); c.Pk[1][2 + s_] = __builtin_bit_cast(bf16x8, w1);
  }
  #pragma unroll
  for (int qb = 0; qb < 2; ++qb) {
    if (qb == 0) tr_reads(c, vbuf, iseq<0, 1, 2, 3, 4, 5, 6, 7, 8, 9, 10, 11, 12, 13, 14, 15>{});
    else         tr_reads(c, vbuf, iseq<16, 17, 18, 19, 20, 21, 22, 23, 24, 25, 26, 27, 28, 29, 30, 31>{});
    if (qb == 0) asm volatile("s_waitcnt lgkmcnt(0)" : W4_V01 :: "memory"); else asm volatile("s_waitcnt lgkmcnt(0)" : W4_V23 :: "memory");
    GAP();
    #pragma unroll
    for (int ks = 2 * qb; ks < 2 * qb + 2; ++ks)
      #pragma unroll
      for (int db = 0; db < 4; ++db) c.Vf[db][ks] = __builtin_shufflevector(c.vlo[db][ks], c.vup[db][ks], 0, 1, 2, 3, 4, 5, 6, 7);
    if (qb == 0) pv_tree_all(c, iseq<0, 1, 2, 3, 4, 5, 6, 7>{}); else pv_tree_all(c, iseq<8, 9, 10, 11, 12, 13, 14, 15>{});
  }
  asm volatile("s_nop 7\n\ts_nop 7" ::: "memory");
}

extern "C" __global__ __launch_bounds__(256, 1)
void attn_main(const unsigned short* __restrict__ Qg, const unsigned short* __restrict__ Kc, const unsigned short* __restrict__ Vc,
               const unsigned short* __restrict__ Kt, const unsigned short* __restrict__ Vt,
               const int* __restrict__ lcnt, const unsigned* __restrict__ lent, unsigned short* __restrict__ Og) {
  extern __shared__ __attribute__((aligned(16))) char lds[];
  const unsigned tid = threadIdx.x, lane = tid & 63u, wid = tid >> 6, l32 = lane & 31u, hi = lane >> 5;
  const float C = 0.12751743082f;
  STAMP(0);
  W4C c;
  c.T = NT; c.THR = THR;
  c.ldsb = (unsigned)(size_t)lds;
  #pragma unroll
  for (int j = 0; j < 4; ++j) c.vkb[j] = c.ldsb + KRING + 256u * l32 + 16u * ((2u * j + hi) ^ (l32 & 7u));
  c.vvb = c.ldsb + VRING + (((lane & 3u) << 3) | (((lane >> 2) & 3u) << 6) | (((lane >> 4) & 1u) << 5) | (hi << 8));
  #pragma unroll
  for (int par = 0; par < 2; ++par) c.vksrc[par] = KRS * (lane >> 4) + 16u * ((lane & 15u) ^ (4u * par + (lane >> 4)));
  c.vvsrc = KRS * ((lane >> 2) & 7u) + 64u * hi + 16u * (lane & 3u);
  unsigned vqsrc[2];
  #pragma unroll
  for (int par = 0; par < 2; ++par) vqsrc[par] = (unsigned)(TQ * 256) * (lane >> 4) + 16u * ((lane & 15u) ^ (4u * par + (lane >> 4)));
  c.m0k = rfl(c.ldsb + KRING + 4096u * wid); c.m0v = rfl(c.ldsb + VRING + 4096u * wid);
  const unsigned qoff = QSTAGE + 16384u * wid;
  c.sofk = rfl(KRS * 16u * wid); c.sofv = rfl(KRS * 16u * wid);

  const unsigned ublk = blockIdx.x;
  const int kvh = (int)(ublk & 7u), rb = (int)(ublk >> 3);
  const int qrow0 = 64 * rb + 16 * (int)rfl(wid);
  const char* Kbase = (const char*)Kc + (size_t)kvh * CC * 256;
  const char* Vbase = (const char*)Vc + (size_t)kvh * CC * 256;
  c.srdK = __builtin_amdgcn_make_buffer_rsrc((void*)Kbase, (short)0, CC * 256, SRD_WORD3);
  c.srdV = __builtin_amdgcn_make_buffer_rsrc((void*)Vbase, (short)0, CC * 256, SRD_WORD3);
  auto dma_k = [&](int tk) {
    const unsigned slot = (unsigned)(tk & 1) * 0x4000u, skv = KTILEB * (unsigned)tk;
    #pragma unroll
    for (int q = 0; q < 4; ++q)
      asm volatile("s_nop 4\n\ts_mov_b32 m0, %0\n\ts_nop 0\n\tbuffer_load_dwordx4 %1, %2, %3 offen lds"
                   :: "s"(rfl(c.m0k + slot + 1024u * q)), "v"(c.vksrc[q & 1]), "s"(c.srdK), "s"(rfl(skv + c.sofk + KRS * 4u * q)) : "m0", "memory");
  };
  auto dma_v = [&](int tv) {
    const unsigned slot = (unsigned)(tv & 1) * 0x4000u, skv = KTILEB * (unsigned)tv;
    #pragma unroll
    for (int q = 0; q < 4; ++q)
      asm volatile("s_nop 4\n\ts_mov_b32 m0, %0\n\ts_nop 0\n\tbuffer_load_dwordx4 %1, %2, %3 offen lds"
                   :: "s"(rfl(c.m0v + slot + 1024u * q)), "v"(c.vvsrc), "s"(c.srdV), "s"(rfl(skv + c.sofv + KRS * 8u * (q >> 1) + 128u * (q & 1))) : "m0", "memory");
  };
  const unsigned m0q = rfl(c.ldsb + QSTAGE + 16384u * wid);
  {
    __amdgpu_buffer_rsrc_t srdQ = __builtin_amdgcn_make_buffer_rsrc((void*)Qg, (short)0, HQ * TQ * 256, SRD_WORD3);
    #pragma unroll
    for (int qp = 0; qp < 16; ++qp)
      asm volatile("s_nop 4\n\ts_mov_b32 m0, %0\n\ts_nop 0\n\tbuffer_load_dwordx4 %1, %2, %3 offen lds"
                   :: "s"(rfl(m0q + 1024u * qp)), "v"(vqsrc[qp & 1]), "s"(srdQ), "s"(rfl((unsigned)(((4 * kvh) * TQ + qrow0 + qp) * 256))) : "m0", "memory");
    dma_k(0); dma_v(0); dma_k(1);
  }
  asm volatile("s_waitcnt vmcnt(12)" ::: "memory");
  {
    unsigned qb4[4];
    #pragma unroll
    for (int j = 0; j < 4; ++j) { qb4[j] = c.vkb[j] + qoff; asm volatile("" : "+v"(qb4[j])); }
    #pragma unroll
    for (int qb = 0; qb < 2; ++qb) {
      bf16x8 qf[8];
      #pragma unroll
      for (int d0 = 0; d0 < 8; ++d0) qf[d0] = q_ldread(qb4[d0 & 3], qb, d0);
      asm volatile("s_waitcnt lgkmcnt(0)" : "+v"(qf[0]), "+v"(qf[1]), "+v"(qf[2]), "+v"(qf[3]), "+v"(qf[4]), "+v"(qf[5]), "+v"(qf[6]), "+v"(qf[7]) :: "memory");
      #pragma unroll
      for (int d0 = 0; d0 < 8; ++d0) {
        u32x4 w;
        #pragma unroll
        for (int e = 0; e < 4; ++e) w[e] = cvtpk(fmul_s((float)qf[d0][2 * e], C), fmul_s((float)qf[d0][2 * e + 1], C));
        q_write(qb, d0, w);
      }
    }
  }
  asm volatile("s_waitcnt vmcnt(0)\n\ts_barrier" ::: "memory");
  k_reads(c, 0u, make_iseq<16>{});
  asm volatile("s_waitcnt lgkmcnt(0)\n\ts_nop 1" ::: "memory");

  c.mhat[0] = c.mhat[1] = 0.f; c.lsum[0] = c.lsum[1] = 0.f; c.fct[0] = c.fct[1] = 1.f; c.pend = false;
  const int qblk0 = 8 * rb + 2 * (int)rfl(wid);
  const int cnt0 = lcnt[qblk0], cnt1 = lcnt[qblk0 + 1];
  const int ntile = ((cnt0 > cnt1 ? cnt0 : cnt1) + 31) >> 5;
  const unsigned stash = OSTAGE + 8192u * wid;
  const unsigned vbufA = QSTAGE + 16384u * wid - VRING, vbufB = KRING + 16384u * wid - VRING;
  const __amdgpu_buffer_rsrc_t srdKt = __builtin_amdgcn_make_buffer_rsrc((void*)((const char*)Kt + (size_t)kvh * TQ * 256), (short)0, TQ * 256, SRD_WORD3);
  const __amdgpu_buffer_rsrc_t srdVt = __builtin_amdgcn_make_buffer_rsrc((void*)((const char*)Vt + (size_t)kvh * TQ * 256), (short)0, TQ * 256, SRD_WORD3);
  auto fill_stash = [&](int j0, const unsigned lane) {
    const unsigned l32 = lane & 31u, hi = lane >> 5;
    const unsigned* entp = lent + ((size_t)qblk0 + hi) * LCAP;
    const int cntl = hi ? cnt1 : cnt0;
    #pragma unroll
    for (int jj = 0; jj < 4; ++jj) {
      const int idx = 32 * (j0 + jj) + (int)l32;
      unsigned e = 0xFFFF0000u;
      if (idx < cntl) e = entp[idx];
      *(unsigned*)(lds + stash + 256u * jj + 4u * lane) = e;
    }
  };
  auto issue_k = [&](int j, const unsigned lane) {
    const unsigned l32 = lane & 31u, hi = lane >> 5;
    const unsigned stj = stash + 256u * (unsigned)(j & 3);
    const unsigned e0 = *(const unsigned*)(lds + stj + 4u * l32), e1 = *(const unsigned*)(lds + stj + 4u * (32u + l32));
    const unsigned v0 = (e0 >> 24) ? 0x7FFFFF00u : (e0 & 0xFFFFu) * 256u + 16u * hi, v1 = (e1 >> 24) ? 0x7FFFFF00u : (e1 & 0xFFFFu) * 256u + 16u * hi;
    kt_loads(v0, v1, srdKt, make_iseq<16>{});
  };
  auto issue_v = [&](int j, const unsigned lane) {
    const unsigned hi = lane >> 5;
    const unsigned stj = stash + 256u * (unsigned)(j & 3);
    const unsigned dst = rfl(c.ldsb + VRING + ((j & 1) ? vbufB : vbufA));
    #pragma unroll
    for (int g = 0; g < 8; ++g) {
      const unsigned kg = *(const unsigned*)(lds + stj + 4u * (8u * g + ((lane >> 2) & 7u)));
      const unsigned vo = (kg >> 24) ? 0x7FFFFF00u : (kg & 0xFFFFu) * 256u + 64u * hi + 16u * (lane & 3u);
      asm volatile("s_nop 4\n\ts_mov_b32 m0, %0\n\ts_nop 0\n\tbuffer_load_dwordx4 %1, %2, 0 offen lds" :: "s"(rfl(dst + 2048u * g)), "v"(vo), "s"(srdVt) : "m0", "memory");
      asm volatile("s_mov_b32 m0, %0\n\ts_nop 0\n\tbuffer_load_dwordx4 %1, %2, %3 offen lds" :: "s"(rfl(dst + 2048u * g + 1024u)), "v"(vo), "s"(srdVt), "s"(rfl(128u)) : "m0", "memory");
    }
  };
  if (ntile > 0) fill_stash(0, lane);
  STAMP(1);
  constexpr int T = NT;
  phaseA<0, true>(c, 0);  phaseB<0, true,  false>(c, 0);
  phaseA<1, false>(c, 1); phaseB<1, false, true>(c, 1);
  for (int t = 2; t < T - 2; t += 2) {
    phaseA<0, false>(c, t);     phaseB<0, false, false>(c, t);
    phaseA<1, false>(c, t + 1); phaseB<1, false, false>(c, t + 1);
  }
  phaseA<0, false>(c, T - 2); phaseB<0, false, false,  0,  1,  true>(c, T - 2);
  phaseA<1, false>(c, T - 1); phaseB<1, false, false,  0,  0,  false>(c, T - 1);
  STAMP(2);
  unsigned lb;
  asm volatile("v_mbcnt_lo_u32_b32 %0, -1, 0\n\tv_mbcnt_hi_u32_b32 %0, -1, %0" : "=v"(lb));
  const unsigned l32b = lb & 31u, hib = lb >> 5;
  if (ntile > 0) { issue_v(0, lb); issue_k(0, lb); }
  {
    const unsigned vslot = (unsigned)((T - 1) & 1) * 0x4000u;
    tr_reads(c, vslot, make_iseq<32>{});
    #pragma unroll
    for (int e = 0; e < 32; ++e) c.S[1][1][e >> 4][e & 15] = __builtin_amdgcn_exp2f(c.S[1][1][e >> 4][e & 15]);
    packs<1>(c, make_iseq<32>{});
    rowsums<1>(c, iseq<8, 9, 10, 11, 12, 13, 14, 15, 24, 25, 26, 27, 28, 29, 30, 31>{});
    #pragma unroll
    for (int qb = 0; qb < 2; ++qb) { const float rs = fadd_s(c.rs0[qb], c.rs1[qb]);
      auto pr = __builtin_amdgcn_permlane32_swap(__float_as_uint(rs), __float_as_uint(rs), false, false);
      c.lsum[qb] = fadd_s(c.lsum[qb], fadd_s(__uint_as_float(pr[0]), __uint_as_float(pr[1]))); }
    asm volatile("s_waitcnt lgkmcnt(0)" : W4_VLO :: "memory"); asm volatile("s_barrier" : W4_VUP :: "memory");
    GAP();
    #pragma unroll
    for (int ks = 0; ks < 4; ++ks)
      #pragma unroll
      for (int db = 0; db < 4; ++db) c.Vf[db][ks] = __builtin_shufflevector(c.vlo[db][ks], c.vup[db][ks], 0, 1, 2, 3, 4, 5, 6, 7);
    asm volatile("s_nop 1" ::: "memory");
    pv_all<false>(c, make_iseq<32>{});
    asm volatile("s_nop 7\n\ts_nop 7" ::: "memory");
  }
  STAMP(3);
  for (int j = 0; j < ntile; ++j)
    tree_tile(c, j, ntile, lb, l32b, hib, lds, stash, (j & 1) ? vbufB : vbufA,
              [&](int jj) { if ((jj & 3) == 0) fill_stash(jj, lb); issue_v(jj, lb); }, [&](int jj) { issue_k(jj, lb); });
  STAMP(4);
  {
    char* stg = lds + OSTAGE + 8192u * wid;
    #pragma unroll
    for (int qb = 0; qb < 2; ++qb) {
      const float il = c.lsum[qb] > 0.f ? __builtin_amdgcn_rcpf(c.lsum[qb]) : 0.f;
      #pragma unroll
      for (int db = 0; db < 4; ++db) {
        float o[16];
        if (qb == 0) { switch (db) { case 0: read_o16<AO_(0, 0)>(o); break; case 1: read_o16<AO_(1, 0)>(o); break; case 2: read_o16<AO_(2, 0)>(o); break; default: read_o16<AO_(3, 0)>(o); break; } }
        else         { switch (db) { case 0: read_o16<AO_(0, 1)>(o); break; case 1: read_o16<AO_(1, 1)>(o); break; case 2: read_o16<AO_(2, 1)>(o); break; default: read_o16<AO_(3, 1)>(o); break; } }
        #pragma unroll
        for (int gq = 0; gq < 4; ++gq) {
          u32x2 w = {cvtpk(o[4 * gq + 0] * il, o[4 * gq + 1] * il), cvtpk(o[4 * gq + 2] * il, o[4 * gq + 3] * il)};
          const unsigned ch = 4u * db + gq;
          *(u32x2*)(stg + 256u * l32b + 16u * (ch ^ (l32b & 15u)) + 8u * hib) = w;
        }
      }
      asm volatile("s_waitcnt lgkmcnt(0)" ::: "memory");
      u32x4 v[8];
      #pragma unroll
      for (int j = 0; j < 8; ++j) { const unsigned r = 4u * j + (lb >> 4); v[j] = *(const u32x4*)(stg + 256u * r + 16u * ((lb & 15u) ^ (r & 15u))); }
      #pragma unroll
      for (int j = 0; j < 8; ++j) {
        char* dst = (char*)Og + ((size_t)((4 * kvh + (int)(lb >> 4)) * TQ + qrow0 + 8 * qb + j)) * 256 + 16u * (lb & 15u);
        *(u32x4*)dst = v[j];
      }
      asm volatile("s_waitcnt lgkmcnt(0)" ::: "memory");
    }
  }
}

__global__ __launch_bounds__(512) void build_lists(const unsigned short* __restrict__ mask, int* __restrict__ lcnt, unsigned* __restrict__ lent) {
  __shared__ unsigned rb[8][64];
  const int qb = blockIdx.x, tid = threadIdx.x, lane = tid & 63, r = tid >> 6;
  const u32x4* mp = (const u32x4*)(mask + (size_t)(8 * qb + r) * TQ) + lane * 4;
  u32x4 m[4];
  #pragma unroll
  for (int k = 0; k < 4; ++k) m[k] = mp[k];
  unsigned bits = 0;
  #pragma unroll
  for (int k = 0; k < 4; ++k)
    #pragma unroll
    for (int e = 0; e < 4; ++e) { bits |= ((m[k][e] & 0xFFFFu) == 0x3F80u ? 1u : 0u) << (8 * k + 2 * e); bits |= ((m[k][e] >> 16) == 0x3F80u ? 1u : 0u) << (8 * k + 2 * e + 1); }
  rb[r][lane] = bits;
  __syncthreads();
  if (r != 0) return;
  unsigned w[8], un = 0;
  #pragma unroll
  for (int k = 0; k < 8; ++k) { w[k] = rb[k][lane]; un |= w[k]; }
  const int cnt = __popc(un);
  int inc = cnt;
  #pragma unroll
  for (int s = 1; s < 64; s <<= 1) { const int o = __shfl_up(inc, s); if (lane >= s) inc += o; }
  unsigned* ent = lent + (size_t)qb * LCAP;
  int pos = inc - cnt;
  while (un) {
    const int b = __ffs(un) - 1; un &= un - 1;
    unsigned rm = 0;
    #pragma unroll
    for (int k = 0; k < 8; ++k) rm |= ((w[k] >> b) & 1u) << k;
    ent[pos++] = (unsigned)(32 * lane + b) | (rm << 16);
  }
  if (lane == 63) lcnt[qb] = inc;
}

extern "C" void kernel_launch(void* const* d_in, const int* in_sizes, int n_in, void* d_out, int out_size, void* d_ws, size_t ws_size, hipStream_t stream) {
  const unsigned short* q  = (const unsigned short*)d_in[0];
  const unsigned short* kc = (const unsigned short*)d_in[1];
  const unsigned short* vc = (const unsigned short*)d_in[2];
  const unsigned short* kt = (const unsigned short*)d_in[3];
  const unsigned short* vt = (const unsigned short*)d_in[4];
  const unsigned short* mk = (const unsigned short*)d_in[5];
  int* lcnt = (int*)((char*)d_ws + WS_CNT);
  unsigned* lent = (unsigned*)((char*)d_ws + WS_ENT);
  (void)hipFuncSetAttribute((const void*)attn_main, hipFuncAttributeMaxDynamicSharedMemorySize, (int)SHM_ATTN);
  hipLaunchKernelGGL(build_lists, dim3(TQ / 8), dim3(512), 0, stream, mk, lcnt, lent);
  hipLaunchKernelGGL(attn_main, dim3(256), dim3(64 * NWAVE), SHM_ATTN, stream, q, kc, vc, kt, vt, (const int*)lcnt, (const unsigned*)lent, (unsigned short*)d_out);
}
```
